```python
import torch

def _softmax(x, axis=-1):
    return torch.softmax(x, dim=axis)
def _gelu(x):
    return torch.nn.functional.gelu(x, approximate="tanh")
def _silu(x):
    return torch.nn.functional.silu(x)
def _sigmoid(x):
    return torch.sigmoid(x)
def _layernorm(x, axis=-1, eps=1e-5):
    m = x.mean(dim=axis, keepdim=True)
    v = x.var(dim=axis, keepdim=True, unbiased=False)
    return (x - m) / torch.sqrt(v + eps)
def _scatter_add(tgt, idx, src, axis=0):
    return tgt.clone().index_add_(axis, idx, src)
def _segment_sum(src, seg, n):
    out = torch.zeros((n,) + src.shape[1:], dtype=src.dtype)
    return out.index_add_(0, seg, src)
def _pad(x, pw):
    flat = tuple(v for pair in reversed(pw) for v in pair)
    return torch.nn.functional.pad(x, flat)

kernel_name = 'sequential_preLN__mha_block_cached__swiglu_ffn__decode'

def setup_inputs():

    _w = (lambda a: (lambda t: t.to(torch.bfloat16) if t.is_floating_point() else t)(torch.as_tensor(a)))
    def _A_setup():
        B, Sq, D = 2, 1, 512
        H, Sk, Dh = 32, 16384, 128
        return {
            "x": _w((lambda *s: torch.randn(*s, dtype=torch.bfloat16) * 0.05)(B, Sq, D)),
            "k_cache": _w((lambda *s: torch.randn(*s, dtype=torch.bfloat16) * 3.2)(B, H, Sk, Dh)),
            "v_cache": _w((lambda *s: torch.randn(*s, dtype=torch.bfloat16) * 0.4)(B, H, Sk, Dh)),
            "wq": _w((lambda *s: torch.randn(*s, dtype=torch.bfloat16) * 3.2)(D, H * Dh) * (D ** -0.5)),
            "wo": _w((lambda *s: torch.randn(*s, dtype=torch.bfloat16) * 0.4)(H * Dh, D) * ((H * Dh) ** -0.5)),
        }
    _A_in = _A_setup()

    def _B_setup():
        B, S, D, F = 2, 1, 512, 1792
        return {
            "x": _w((lambda *s: torch.randn(*s, dtype=torch.bfloat16) * 0.05)(B, S, D)),
            "w_gate": _w((lambda *s: torch.randn(*s, dtype=torch.bfloat16) * 0.05)(D, F)),
            "w_up": _w((lambda *s: torch.randn(*s, dtype=torch.bfloat16) * 0.05)(D, F)),
            "w_down": _w((lambda *s: torch.randn(*s, dtype=torch.bfloat16) * 0.05)(F, D)),
        }
    _B_in = _B_setup()

    return {
        "A_x": _A_in["x"],
        "A_k_cache": _A_in["k_cache"],
        "A_v_cache": _A_in["v_cache"],
        "A_wq": _A_in["wq"],
        "A_wo": _A_in["wo"],
        "B_x": _B_in["x"],
        "B_w_gate": _B_in["w_gate"],
        "B_w_up": _B_in["w_up"],
        "B_w_down": _B_in["w_down"],
    }

def reference(A_x, A_k_cache, A_v_cache, A_wq, A_wo, B_x, B_w_gate, B_w_up, B_w_down):
    def _A_ref(x, k_cache, v_cache, wq, wo):
        B, Sq, D = x.shape
        H, Dh = 32, 128
        q = (lambda x, *d: x.permute(*d))((x @ wq).reshape(B, Sq, H, Dh), 0, 2, 1, 3)
        scores = torch.einsum("bhqd,bhkd->bhqk", q, k_cache) * (Dh ** -0.5)
        p = _softmax(scores, axis=-1)
        o = torch.einsum("bhqk,bhkd->bhqd", p, v_cache)
        return (lambda x, *d: x.permute(*d))(o, 0, 2, 1, 3).reshape(B, Sq, H * Dh) @ wo

    def _B_ref(x, w_gate, w_up, w_down):
        g = _silu(x @ w_gate)
        u = x @ w_up
        return (g * u) @ w_down

    x = A_x
    h = x + _A_ref(_layernorm(x, axis=-1), A_k_cache, A_v_cache, A_wq, A_wo)
    h2 = h + _B_ref(_layernorm(h, axis=-1), B_w_gate, B_w_up, B_w_down)
    return h2
```

```cpp
#include <hip/hip_runtime.h>
#include <hip/hip_bf16.h>
#include <stdint.h>

#define MIMIC 1

typedef unsigned int u32;
typedef unsigned short u16;
typedef __attribute__((ext_vector_type(4))) unsigned u32x4;
typedef __attribute__((ext_vector_type(2))) unsigned u32x2;
typedef __attribute__((ext_vector_type(4))) float f32x4;
typedef __attribute__((ext_vector_type(2))) __bf16 bf16x2;

constexpr int NB = 2, DM = 512, NH = 32, SK = 16384, DH = 128, FF = 1792;
constexpr int NSPLIT = 8;
constexpr int AW = 4;
constexpr int KPS = SK / NSPLIT;
constexpr float SCALE = 0.08838834764831845f;
constexpr float LOG2E = 1.4426950408889634f;

__device__ __forceinline__ float bf2f(u16 v) { return __uint_as_float((u32)v << 16); }
__device__ __forceinline__ float lo2f(u32 v) { return __uint_as_float(v << 16); }
__device__ __forceinline__ float hi2f(u32 v) { return __uint_as_float(v & 0xffff0000u); }
__device__ __forceinline__ float rbf(float x) { return (float)(__bf16)x; }
__device__ __forceinline__ float mrbf(float x) { return MIMIC ? rbf(x) : x; }
__device__ __forceinline__ u16 f2bf(float x) { __bf16 b = (__bf16)x; return __builtin_bit_cast(u16, b); }
__device__ __forceinline__ float dot2(u32 a, u32 b, float acc) {
  return __builtin_amdgcn_fdot2_f32_bf16(__builtin_bit_cast(bf16x2, a), __builtin_bit_cast(bf16x2, b), acc, false);
}
template <int CTRL> __device__ __forceinline__ float dpp(float x) {
  return __builtin_bit_cast(float, __builtin_amdgcn_mov_dpp(__builtin_bit_cast(int, x), CTRL, 0xf, 0xf, true));
}
__device__ __forceinline__ float row16_sum(float x) {
  x += dpp<0x128>(x);
  x += dpp<0x124>(x);
  x += dpp<0x122>(x);
  x += dpp<0x121>(x);
  return x;
}
__device__ __forceinline__ float wave_sum(float x) {
  x = row16_sum(x);
  x += __shfl_xor(x, 16);
  x += __shfl_xor(x, 32);
  return x;
}

__device__ __forceinline__ void layernorm2(const float* xs, float* ln, float* red, int tid) {
  float a0 = xs[2 * tid], a1 = xs[2 * tid + 1], b0 = xs[512 + 2 * tid], b1 = xs[512 + 2 * tid + 1];
  float s0 = wave_sum(a0 + a1), s1 = wave_sum(b0 + b1);
  int w = tid >> 6;
  if ((tid & 63) == 0) { red[w] = s0; red[4 + w] = s1; }
  __syncthreads();
  float m0 = (red[0] + red[1] + red[2] + red[3]) * (1.f / 512.f);
  float m1 = (red[4] + red[5] + red[6] + red[7]) * (1.f / 512.f);
  __syncthreads();
  float v0 = wave_sum((a0 - m0) * (a0 - m0) + (a1 - m0) * (a1 - m0));
  float v1 = wave_sum((b0 - m1) * (b0 - m1) + (b1 - m1) * (b1 - m1));
  if ((tid & 63) == 0) { red[w] = v0; red[4 + w] = v1; }
  __syncthreads();
  v0 = (red[0] + red[1] + red[2] + red[3]) * (1.f / 512.f);
  v1 = (red[4] + red[5] + red[6] + red[7]) * (1.f / 512.f);
  m0 = mrbf(m0); m1 = mrbf(m1);
  v0 = mrbf(v0); v1 = mrbf(v1);
  float d0 = mrbf(sqrtf(mrbf(v0 + 1e-5f))), d1 = mrbf(sqrtf(mrbf(v1 + 1e-5f)));
  ln[2 * tid] = mrbf(mrbf(a0 - m0) / d0);
  ln[2 * tid + 1] = mrbf(mrbf(a1 - m0) / d0);
  ln[512 + 2 * tid] = mrbf(mrbf(b0 - m1) / d1);
  ln[512 + 2 * tid + 1] = mrbf(mrbf(b1 - m1) / d1);
  __syncthreads();
}

__global__ void __launch_bounds__(256) k_qproj(const u16* __restrict__ x, const u16* __restrict__ wq, float* __restrict__ qout) {
  __shared__ float xs[1024], ln[1024], red[8];
  __shared__ float part[32][2][64 + 4];
  int tid = threadIdx.x;
  for (int i = tid; i < 1024; i += 256) xs[i] = bf2f(x[i]);
  __syncthreads();
  layernorm2(xs, ln, red, tid);
  int col0 = blockIdx.x * 64;
  int cg = tid & 7, rg = tid >> 3;
  float acc0[8] = {0}, acc1[8] = {0};
#pragma unroll
  for (int i = 0; i < 16; ++i) {
    int row = rg + 32 * i;
    u32x4 w = *reinterpret_cast<const u32x4*>(wq + (size_t)row * 4096 + col0 + cg * 8);
    float l0 = ln[row], l1 = ln[512 + row];
#pragma unroll
    for (int k = 0; k < 4; ++k) {
      float wl = lo2f(w[k]), wh = hi2f(w[k]);
      acc0[2 * k] += l0 * wl; acc0[2 * k + 1] += l0 * wh;
      acc1[2 * k] += l1 * wl; acc1[2 * k + 1] += l1 * wh;
    }
  }
#pragma unroll
  for (int k = 0; k < 8; ++k) { part[rg][0][cg * 8 + k] = acc0[k]; part[rg][1][cg * 8 + k] = acc1[k]; }
  __syncthreads();
  if (tid < 128) {
    int b = tid >> 6, c = tid & 63;
    float s = 0.f;
    for (int r = 0; r < 32; ++r) s += part[r][b][c];
    qout[b * 4096 + col0 + c] = s;
  }
}

__global__ void __launch_bounds__(AW * 64) k_attn(const u16* __restrict__ kc, const u16* __restrict__ vc, const float* __restrict__ q,
                                                  float* __restrict__ pml, float* __restrict__ po) {
  __shared__ float mrg[AW * 4][DH + 4];
  const int bh = blockIdx.x / NSPLIT, sp = blockIdx.x % NSPLIT;
  const int tid = threadIdx.x, lane = tid & 63, wave = tid >> 6, r = lane >> 4, c = lane & 15;
  u32 qp[4];
  {
    const float* qq = q + (size_t)bh * DH + 8 * c;
#pragma unroll
    for (int k = 0; k < 4; ++k) qp[k] = (u32)f2bf(qq[2 * k]) | ((u32)f2bf(qq[2 * k + 1]) << 16);
  }
  const size_t base = ((size_t)bh * SK + (size_t)sp * KPS) * DH;
  const u16* kb = kc + base + (size_t)lane * 8;
  const u16* vb = vc + base + (size_t)lane * 8;
  constexpr int NT = KPS / 16 / AW;
  u32x4 K0[4], V0[4], K1[4], V1[4];
  float m = -INFINITY, l = 0.f, acc[8];
#pragma unroll
  for (int k = 0; k < 8; ++k) acc[k] = 0.f;

#define ISSUE(KB, VB, i_) do { const size_t off_ = (size_t)((i_) * AW + wave) * 16 * DH; \
    _Pragma("unroll") for (int t_ = 0; t_ < 4; ++t_) KB[t_] = __builtin_nontemporal_load(reinterpret_cast<const u32x4*>(kb + off_ + t_ * 4 * DH)); \
    _Pragma("unroll") for (int t_ = 0; t_ < 4; ++t_) VB[t_] = __builtin_nontemporal_load(reinterpret_cast<const u32x4*>(vb + off_ + t_ * 4 * DH)); } while (0)
#define COMPUTE(KB, VB) do { float s_[4]; \
    _Pragma("unroll") for (int t_ = 0; t_ < 4; ++t_) { float d_ = 0.f; \
      _Pragma("unroll") for (int k_ = 0; k_ < 4; ++k_) d_ = dot2(KB[t_][k_], qp[k_], d_); \
      d_ = row16_sum(d_); \
      s_[t_] = mrbf(mrbf(d_) * SCALE) * LOG2E; } \
    float mn_ = fmaxf(fmaxf(fmaxf(s_[0], s_[1]), fmaxf(s_[2], s_[3])), m); \
    float al_ = __builtin_amdgcn_exp2f(m - mn_); m = mn_; \
    float p_[4]; \
    _Pragma("unroll") for (int t_ = 0; t_ < 4; ++t_) p_[t_] = __builtin_amdgcn_exp2f(s_[t_] - mn_); \
    l = l * al_ + ((p_[0] + p_[1]) + (p_[2] + p_[3])); \
    _Pragma("unroll") for (int k_ = 0; k_ < 8; ++k_) acc[k_] *= al_; \
    _Pragma("unroll") for (int t_ = 0; t_ < 4; ++t_) { \
      _Pragma("unroll") for (int k_ = 0; k_ < 4; ++k_) { \
        acc[2 * k_] = fmaf(p_[t_], lo2f(VB[t_][k_]), acc[2 * k_]); \
        acc[2 * k_ + 1] = fmaf(p_[t_], hi2f(VB[t_][k_]), acc[2 * k_ + 1]); } } } while (0)

  ISSUE(K0, V0, 0);
  for (int i = 0; i < NT; i += 2) {
    ISSUE(K1, V1, i + 1);
    COMPUTE(K0, V0);
    if (i + 2 < NT) ISSUE(K0, V0, i + 2);
    COMPUTE(K1, V1);
  }
#undef ISSUE
#undef COMPUTE
  {
    float* rec = mrg[wave * 4 + r];
#pragma unroll
    for (int k = 0; k < 8; ++k) rec[4 + 8 * c + k] = acc[k];
    if (c == 0) { rec[0] = m; rec[1] = l; }
  }
  __syncthreads();
  if (tid < DH) {
    float M = -INFINITY;
#pragma unroll
    for (int w = 0; w < AW * 4; ++w) M = fmaxf(M, mrg[w][0]);
    float L = 0.f, o = 0.f;
#pragma unroll
    for (int w = 0; w < AW * 4; ++w) {
      float wt = __builtin_amdgcn_exp2f(mrg[w][0] - M);
      L += wt * mrg[w][1];
      o += wt * mrg[w][4 + tid];
    }
    po[((size_t)bh * NSPLIT + sp) * DH + tid] = o;
    if (tid == 0) { pml[((size_t)bh * NSPLIT + sp) * 2] = M; pml[((size_t)bh * NSPLIT + sp) * 2 + 1] = L; }
  }
}

__global__ void __launch_bounds__(128) k_combine(const float* __restrict__ pml, const float* __restrict__ po, float* __restrict__ o) {
  int bh = blockIdx.x, d = threadIdx.x;
  float M = -INFINITY;
  for (int s = 0; s < NSPLIT; ++s) M = fmaxf(M, pml[(bh * NSPLIT + s) * 2]);
  float L = 0.f, a = 0.f;
  for (int s = 0; s < NSPLIT; ++s) {
    float wt = __builtin_amdgcn_exp2f(pml[(bh * NSPLIT + s) * 2] - M);
    L += wt * pml[(bh * NSPLIT + s) * 2 + 1];
    a += wt * po[((size_t)bh * NSPLIT + s) * DH + d];
  }
  o[bh * DH + d] = mrbf(a / L);
}

__global__ void __launch_bounds__(256) k_wo(const u16* __restrict__ x, const float* __restrict__ o, const u16* __restrict__ wo, float* __restrict__ h) {
  __shared__ float red[4][16];
  int tid = threadIdx.x, col0 = blockIdx.x * 8;
  float a0[8] = {0}, a1[8] = {0};
#pragma unroll
  for (int i = 0; i < 16; ++i) {
    int row = tid + 256 * i;
    u32x4 w = *reinterpret_cast<const u32x4*>(wo + (size_t)row * 512 + col0);
    float l0 = o[row], l1 = o[4096 + row];
#pragma unroll
    for (int k = 0; k < 4; ++k) {
      float wl = lo2f(w[k]), wh = hi2f(w[k]);
      a0[2 * k] += l0 * wl; a0[2 * k + 1] += l0 * wh;
      a1[2 * k] += l1 * wl; a1[2 * k + 1] += l1 * wh;
    }
  }
#pragma unroll
  for (int k = 0; k < 8; ++k) { a0[k] = wave_sum(a0[k]); a1[k] = wave_sum(a1[k]); }
  if ((tid & 63) == 0) {
#pragma unroll
    for (int k = 0; k < 8; ++k) { red[tid >> 6][k] = a0[k]; red[tid >> 6][8 + k] = a1[k]; }
  }
  __syncthreads();
  if (tid < 16) {
    float s = red[0][tid] + red[1][tid] + red[2][tid] + red[3][tid];
    int b = tid >> 3, cc = col0 + (tid & 7);
    h[b * 512 + cc] = mrbf(bf2f(x[b * 512 + cc]) + mrbf(s));
  }
}

__global__ void __launch_bounds__(256) k_gateup(const float* __restrict__ h, const u16* __restrict__ wg, const u16* __restrict__ wu, float* __restrict__ act) {
  __shared__ float xs[1024], ln[1024], red[8];
  __shared__ float part[4][4][32];
  int tid = threadIdx.x;
  for (int i = tid; i < 1024; i += 256) xs[i] = h[i];
  __syncthreads();
  layernorm2(xs, ln, red, tid);
  int col0 = blockIdx.x * 16;
  int cg = tid & 1, rg = tid >> 1;
  float g0[8] = {0}, g1[8] = {0}, u0[8] = {0}, u1[8] = {0};
#pragma unroll
  for (int i = 0; i < 4; ++i) {
    int row = rg + 128 * i;
    u32x4 w = *reinterpret_cast<const u32x4*>(wg + (size_t)row * FF + col0 + cg * 8);
    u32x4 w2 = *reinterpret_cast<const u32x4*>(wu + (size_t)row * FF + col0 + cg * 8);
    float l0 = ln[row], l1 = ln[512 + row];
#pragma unroll
    for (int k = 0; k < 4; ++k) {
      float wl = lo2f(w[k]), wh = hi2f(w[k]);
      g0[2 * k] += l0 * wl; g0[2 * k + 1] += l0 * wh;
      g1[2 * k] += l1 * wl; g1[2 * k + 1] += l1 * wh;
      wl = lo2f(w2[k]); wh = hi2f(w2[k]);
      u0[2 * k] += l0 * wl; u0[2 * k + 1] += l0 * wh;
      u1[2 * k] += l1 * wl; u1[2 * k + 1] += l1 * wh;
    }
  }
#pragma unroll
  for (int k = 0; k < 8; ++k) {
#pragma unroll
    for (int s = 2; s < 64; s <<= 1) {
      g0[k] += __shfl_xor(g0[k], s); g1[k] += __shfl_xor(g1[k], s);
      u0[k] += __shfl_xor(u0[k], s); u1[k] += __shfl_xor(u1[k], s);
    }
  }
  if ((tid & 63) < 2) {
    int w = tid >> 6;
#pragma unroll
    for (int k = 0; k < 8; ++k) {
      part[w][0][cg * 8 + k] = g0[k]; part[w][1][cg * 8 + k] = g1[k];
      part[w][2][cg * 8 + k] = u0[k]; part[w][3][cg * 8 + k] = u1[k];
    }
  }
  __syncthreads();
  if (tid < 32) {
    int b = tid >> 4, cc = tid & 15;
    float g = part[0][b][cc] + part[1][b][cc] + part[2][b][cc] + part[3][b][cc];
    float u = part[0][2 + b][cc] + part[1][2 + b][cc] + part[2][2 + b][cc] + part[3][2 + b][cc];
    g = mrbf(g); u = mrbf(u);
    float sg = mrbf(g / (1.f + __expf(-g)));
    act[b * FF + col0 + cc] = mrbf(sg * u);
  }
}

__global__ void __launch_bounds__(256) k_down(const float* __restrict__ h, const float* __restrict__ act, const u16* __restrict__ wd, u16* __restrict__ out) {
  __shared__ float red[4][16];
  int tid = threadIdx.x, col0 = blockIdx.x * 8;
  float a0[8] = {0}, a1[8] = {0};
#pragma unroll
  for (int i = 0; i < 7; ++i) {
    int row = tid + 256 * i;
    u32x4 w = *reinterpret_cast<const u32x4*>(wd + (size_t)row * 512 + col0);
    float l0 = act[row], l1 = act[FF + row];
#pragma unroll
    for (int k = 0; k < 4; ++k) {
      float wl = lo2f(w[k]), wh = hi2f(w[k]);
      a0[2 * k] += l0 * wl; a0[2 * k + 1] += l0 * wh;
      a1[2 * k] += l1 * wl; a1[2 * k + 1] += l1 * wh;
    }
  }
#pragma unroll
  for (int k = 0; k < 8; ++k) { a0[k] = wave_sum(a0[k]); a1[k] = wave_sum(a1[k]); }
  if ((tid & 63) == 0) {
#pragma unroll
    for (int k = 0; k < 8; ++k) { red[tid >> 6][k] = a0[k]; red[tid >> 6][8 + k] = a1[k]; }
  }
  __syncthreads();
  if (tid < 16) {
    float s = red[0][tid] + red[1][tid] + red[2][tid] + red[3][tid];
    int b = tid >> 3, cc = col0 + (tid & 7);
    out[b * 512 + cc] = f2bf(h[b * 512 + cc] + mrbf(s));
  }
}

extern "C" void kernel_launch(void* const* d_in, const int* in_sizes, int n_in,
                              void* d_out, int out_size, void* d_ws, size_t ws_size,
                              hipStream_t stream) {
  const u16* x = (const u16*)d_in[0];
  const u16* kc = (const u16*)d_in[1];
  const u16* vc = (const u16*)d_in[2];
  const u16* wq = (const u16*)d_in[3];
  const u16* wo = (const u16*)d_in[4];
  const u16* wg = (const u16*)d_in[6];
  const u16* wu = (const u16*)d_in[7];
  const u16* wd = (const u16*)d_in[8];
  float* ws = (float*)d_ws;
  float* q = ws;
  float* pml = ws + 8192;
  float* po = ws + 16384;
  float* o = po + 64 * NSPLIT * DH;
  float* h = o + 8192;
  float* act = h + 1024;
  k_qproj<<<64, 256, 0, stream>>>(x, wq, q);
  k_attn<<<64 * NSPLIT, AW * 64, 0, stream>>>(kc, vc, q, pml, po);
  k_combine<<<64, 128, 0, stream>>>(pml, po, o);
  k_wo<<<64, 256, 0, stream>>>(x, o, wo, h);
  k_gateup<<<112, 256, 0, stream>>>(h, wg, wu, act);
  k_down<<<64, 256, 0, stream>>>(h, act, wd, (u16*)d_out);
}
```

```cpp
#include <hip/hip_runtime.h>
#include <hip/hip_bf16.h>
#include <stdint.h>

typedef unsigned int u32;
typedef unsigned short u16;
typedef unsigned long long u64;
typedef __attribute__((ext_vector_type(4))) unsigned u32x4;
typedef __attribute__((ext_vector_type(4))) float f32x4;
typedef __attribute__((ext_vector_type(2))) __bf16 bf16x2;

constexpr int NB = 2, DM = 512, NH = 32, SK = 16384, DH = 128, FF = 1792;
constexpr int NSPLIT = 4;
constexpr int NW = 8;
constexpr int NTHR = NW * 64;
constexpr int KPW = SK / NSPLIT / NW;
constexpr int TK = 32;
constexpr int NT = KPW / TK;
constexpr float SCALE = 0.08838834764831845f;
constexpr float LOG2E = 1.4426950408889634f;
constexpr unsigned SPIN_CAP = 1u << 22;

constexpr size_t WS_E = 0;
constexpr size_t WS_ML = 64;
constexpr size_t WS_PART = WS_ML + 64 * 64;
constexpr size_t WS_HG = WS_PART + 2 * 128 * 512;
constexpr size_t WS_AG = WS_HG + 512;

__device__ __forceinline__ float bf2f(u32 v) { return __uint_as_float(v << 16); }
__device__ __forceinline__ float lo2f(u32 v) { return __uint_as_float(v << 16); }
__device__ __forceinline__ float hi2f(u32 v) { return __uint_as_float(v & 0xffff0000u); }
__device__ __forceinline__ float rbf(float x) { return (float)(__bf16)x; }
__device__ __forceinline__ u32 f2bf(float x) { __bf16 b = (__bf16)x; return (u32)__builtin_bit_cast(u16, b); }
__device__ __forceinline__ float dot2(u32 a, u32 b, float acc) {
  return __builtin_amdgcn_fdot2_f32_bf16(__builtin_bit_cast(bf16x2, a), __builtin_bit_cast(bf16x2, b), acc, false);
}
template <int CTRL> __device__ __forceinline__ float dpp(float x) {
  return __builtin_bit_cast(float, __builtin_amdgcn_mov_dpp(__builtin_bit_cast(int, x), CTRL, 0xf, 0xf, true));
}
constexpr int XOR1 = 0xB1, XOR2 = 0x4E, XOR7 = 0x141, ROR8 = 0x128, ROR4 = 0x124, ROR2 = 0x122, ROR1 = 0x121;
__device__ __forceinline__ float xrow_sum(float x) {
  auto s = __builtin_amdgcn_permlane16_swap(__float_as_uint(x), __float_as_uint(x), false, false);
  x = __uint_as_float(s[0]) + __uint_as_float(s[1]);
  auto t = __builtin_amdgcn_permlane32_swap(__float_as_uint(x), __float_as_uint(x), false, false);
  return __uint_as_float(t[0]) + __uint_as_float(t[1]);
}
__device__ __forceinline__ float xrow_max(float x) {
  auto s = __builtin_amdgcn_permlane16_swap(__float_as_uint(x), __float_as_uint(x), false, false);
  x = fmaxf(__uint_as_float(s[0]), __uint_as_float(s[1]));
  auto t = __builtin_amdgcn_permlane32_swap(__float_as_uint(x), __float_as_uint(x), false, false);
  return fmaxf(__uint_as_float(t[0]), __uint_as_float(t[1]));
}
__device__ __forceinline__ float wave_sum(float x) {
  x += dpp<ROR8>(x); x += dpp<ROR4>(x); x += dpp<ROR2>(x); x += dpp<ROR1>(x);
  return xrow_sum(x);
}
__device__ __forceinline__ float wave_max(float x) {
  x = fmaxf(x, dpp<ROR8>(x)); x = fmaxf(x, dpp<ROR4>(x)); x = fmaxf(x, dpp<ROR2>(x)); x = fmaxf(x, dpp<ROR1>(x));
  return xrow_max(x);
}
__device__ __forceinline__ void st_gran(u64* p, u32 tag, u32 val) {
  __hip_atomic_store(p, ((u64)tag << 32) | (u64)val, __ATOMIC_RELAXED, __HIP_MEMORY_SCOPE_AGENT);
}
__device__ __forceinline__ u64 ld_gran(const u64* p) {
  return __hip_atomic_load(p, __ATOMIC_RELAXED, __HIP_MEMORY_SCOPE_AGENT);
}
template <int N> __device__ __forceinline__ void poll_gran(const u64* const (&p)[N], u32 tag, u32 (&v)[N]) {
  for (unsigned spins = 0;; ++spins) {
    bool ok = true;
#pragma unroll
    for (int k = 0; k < N; ++k) { u64 g = ld_gran(p[k]); v[k] = (u32)g; ok &= ((u32)(g >> 32) == tag); }
    if (__all(ok) || spins > SPIN_CAP) break;
    __builtin_amdgcn_s_sleep(1);
  }
}

__device__ __forceinline__ void wave_ln(const float (&x)[8], float (&y)[8]) {
  float s = ((x[0] + x[1]) + (x[2] + x[3])) + ((x[4] + x[5]) + (x[6] + x[7]));
  float m = wave_sum(s) * (1.f / 512.f);
  float v = 0.f;
#pragma unroll
  for (int k = 0; k < 8; ++k) v += (x[k] - m) * (x[k] - m);
  v = wave_sum(v) * (1.f / 512.f);
  m = rbf(m); v = rbf(v);
  float d = rbf(sqrtf(rbf(v + 1e-5f)));
#pragma unroll
  for (int k = 0; k < 8; ++k) y[k] = rbf(rbf(x[k] - m) / d);
}

#define SB() __builtin_amdgcn_sched_barrier(0)
constexpr int L_SC = 0;
constexpr int L_QP = L_SC + NW * 512;
constexpr int L_RED = L_QP + NW * 128;
constexpr int L_TOT = L_RED + 256;

__global__ void __launch_bounds__(NTHR, 2) fused_kernel(const u16* __restrict__ x, const u16* __restrict__ kc, const u16* __restrict__ vc,
    const u16* __restrict__ wq, const u16* __restrict__ wo, const u16* __restrict__ wg, const u16* __restrict__ wu,
    const u16* __restrict__ wd, u16* __restrict__ out, u64* __restrict__ ws) {
  __shared__ __attribute__((aligned(16))) float lds[L_TOT];
  const int tid = threadIdx.x, lane = tid & 63, wave = __builtin_amdgcn_readfirstlane(tid >> 6), r = lane >> 4, c = lane & 15;
  const int bid = blockIdx.x, xcd = bid & 7, idx = bid >> 3;
  const int head = xcd * 4 + (idx & 3), b = (idx >> 2) & 1, split = idx >> 3;
  const int bh = b * NH + head;
  const int role = idx < 8 ? 0 : (idx < 16 ? 1 : (idx < 30 ? 2 : 3));

  const u32 epoch = (u32)__hip_atomic_load((const u32*)(ws + WS_E), __ATOMIC_RELAXED, __HIP_MEMORY_SCOPE_AGENT);
  u32x4 xv = *reinterpret_cast<const u32x4*>(x + b * DM + 8 * lane);
  SB();
  u32x4 W[16];
  {
    const u16* p = wq + (size_t)(64 * wave + r) * 4096 + head * DH + 8 * c;
#pragma unroll
    for (int i = 0; i < 16; ++i) W[i] = *reinterpret_cast<const u32x4*>(p + (size_t)i * 4 * 4096);
  }

  const size_t base = ((size_t)bh * SK + (size_t)split * (SK / NSPLIT) + (size_t)wave * TK) * DH + (size_t)lane * 8;
  const u16* kb = kc + base;
  const u16* vb = vc + base;
  u32x4 A[8], Bf[8];
#define ISSUE(BUF, j_) do { const u16* s_ = ((j_) < NT ? kb : vb) + (size_t)((j_) & (NT - 1)) * (NW * TK * DH); \
    _Pragma("unroll") for (int t_ = 0; t_ < 8; ++t_) BUF[t_] = __builtin_nontemporal_load(reinterpret_cast<const u32x4*>(s_ + t_ * 4 * DH)); } while (0)
  SB();
  ISSUE(A, 0);
  ISSUE(Bf, 1);
  SB();

  float* sc = lds + L_SC + wave * 512;
  {
    float xf[8], ln[8];
#pragma unroll
    for (int k = 0; k < 4; ++k) { xf[2 * k] = lo2f(xv[k]); xf[2 * k + 1] = hi2f(xv[k]); }
    wave_ln(xf, ln);
    *reinterpret_cast<f32x4*>(sc + 8 * lane) = f32x4{ln[0], ln[1], ln[2], ln[3]};
    *reinterpret_cast<f32x4*>(sc + 8 * lane + 4) = f32x4{ln[4], ln[5], ln[6], ln[7]};
  }
  float qa[8];
#pragma unroll
  for (int k = 0; k < 8; ++k) qa[k] = 0.f;
#pragma unroll
  for (int i = 0; i < 16; ++i) {
    float l = sc[64 * wave + 4 * i + r];
#pragma unroll
    for (int k = 0; k < 4; ++k) { qa[2 * k] = fmaf(l, lo2f(W[i][k]), qa[2 * k]); qa[2 * k + 1] = fmaf(l, hi2f(W[i][k]), qa[2 * k + 1]); }
  }
#pragma unroll
  for (int k = 0; k < 8; ++k) qa[k] = xrow_sum(qa[k]);
  if (r == 0) {
    *reinterpret_cast<f32x4*>(lds + L_QP + wave * 128 + 8 * c) = f32x4{qa[0], qa[1], qa[2], qa[3]};
    *reinterpret_cast<f32x4*>(lds + L_QP + wave * 128 + 8 * c + 4) = f32x4{qa[4], qa[5], qa[6], qa[7]};
  }
  {
    const u16* p = wo + (size_t)(head * DH + 16 * wave) * DM + 8 * lane;
#pragma unroll
    for (int i = 0; i < 16; ++i) W[i] = *reinterpret_cast<const u32x4*>(p + (size_t)i * DM);
  }
  u32x4 RW[4];
  int rcol0;
  {
    const u16* p; size_t st;
    if (role == 0) {
      rcol0 = 8 * (xcd * 8 + idx);
      p = wd + (size_t)tid * DM + rcol0; st = (size_t)512 * DM;
    } else if (role == 2) {
      rcol0 = 16 * (xcd * 14 + idx - 16);
      const int tt = tid & 255;
      p = (wave < 4 ? wg : wu) + (size_t)(tt >> 1) * FF + rcol0 + 8 * (tt & 1); st = (size_t)128 * FF;
    } else {
      rcol0 = 8 * (xcd * 8 + ((idx - 8) & 7));
      p = wd; st = 0;
    }
#pragma unroll
    for (int i = 0; i < 3; ++i) RW[i] = *reinterpret_cast<const u32x4*>(p + (size_t)i * st);
    RW[3] = *reinterpret_cast<const u32x4*>(p + ((role == 0 && tid >= 256) ? (size_t)0 : 3 * st));
  }
  const u32 tag = epoch + 1u;
  u64* part_p = ws + WS_PART + ((size_t)b * 128 + head * 4 + split) * 512 + tid;
  u64* ml_p = ws + WS_ML + (size_t)bh * 64 + (split * NW + wave) * 2;
  st_gran(part_p, epoch, 0u);
  if (lane < 2) st_gran(ml_p + lane, epoch, 0u);
  if (role == 1 && tid < 8) st_gran(ws + WS_HG + rcol0 + tid, epoch, 0u);
  if (role == 2 && tid < 16) st_gran(ws + WS_AG + rcol0 + tid, epoch, 0u);

  __syncthreads();
  u32 qp[4];
  {
    float q[8];
#pragma unroll
    for (int k = 0; k < 8; ++k) q[k] = 0.f;
#pragma unroll
    for (int w = 0; w < NW; ++w) {
      f32x4 a0 = *reinterpret_cast<const f32x4*>(lds + L_QP + w * 128 + 8 * c);
      f32x4 a1 = *reinterpret_cast<const f32x4*>(lds + L_QP + w * 128 + 8 * c + 4);
#pragma unroll
      for (int k = 0; k < 4; ++k) { q[k] += a0[k]; q[4 + k] += a1[k]; }
    }
#pragma unroll
    for (int k = 0; k < 4; ++k) qp[k] = f2bf(q[2 * k]) | (f2bf(q[2 * k + 1]) << 16);
  }

  float mx = -INFINITY;
#define COMPUTE_K(BUF, j_) do { float x_[8]; \
    _Pragma("unroll") for (int t_ = 0; t_ < 8; ++t_) { float d_ = dot2(BUF[t_][0], qp[0], 0.f); d_ = dot2(BUF[t_][1], qp[1], d_); \
      d_ = dot2(BUF[t_][2], qp[2], d_); x_[t_] = dot2(BUF[t_][3], qp[3], d_); } \
    _Pragma("unroll") for (int h_ = 0; h_ < 4; ++h_) { float t1_ = x_[h_] + dpp<ROR8>(x_[h_]), t2_ = x_[h_ + 4] + dpp<ROR8>(x_[h_ + 4]); x_[h_] = (c & 8) ? t2_ : t1_; } \
    _Pragma("unroll") for (int h_ = 0; h_ < 2; ++h_) { float t1_ = x_[h_] + dpp<XOR7>(x_[h_]), t2_ = x_[h_ + 2] + dpp<XOR7>(x_[h_ + 2]); x_[h_] = (c & 4) ? t2_ : t1_; } \
    { float t1_ = x_[0] + dpp<XOR2>(x_[0]), t2_ = x_[1] + dpp<XOR2>(x_[1]); x_[0] = (c & 2) ? t2_ : t1_; } \
    x_[0] += dpp<XOR1>(x_[0]); \
    float s_ = rbf(rbf(x_[0]) * SCALE) * LOG2E; \
    mx = fmaxf(mx, s_); \
    sc[(j_) * 32 + r * 8 + (c >> 1)] = s_; } while (0)

  SB();
  COMPUTE_K(A, 0); SB(); ISSUE(A, 2); SB();
  COMPUTE_K(Bf, 1); SB(); ISSUE(Bf, 3); SB();
#pragma unroll 1
  for (int j = 2; j < NT; j += 2) {
    COMPUTE_K(A, j); SB(); ISSUE(A, j + 2); SB();
    COMPUTE_K(Bf, j + 1); SB(); ISSUE(Bf, j + 3); SB();
  }
  const float mw = wave_max(mx);
  float lw;
  {
    f32x4 s0 = *reinterpret_cast<const f32x4*>(sc + 8 * lane), s1 = *reinterpret_cast<const f32x4*>(sc + 8 * lane + 4);
    float l = 0.f;
#pragma unroll
    for (int k = 0; k < 4; ++k) { s0[k] = __builtin_amdgcn_exp2f(s0[k] - mw); s1[k] = __builtin_amdgcn_exp2f(s1[k] - mw); l += s0[k] + s1[k]; }
    *reinterpret_cast<f32x4*>(sc + 8 * lane) = s0;
    *reinterpret_cast<f32x4*>(sc + 8 * lane + 4) = s1;
    lw = wave_sum(l);
  }
  if (lane < 2) st_gran(ml_p + lane, tag, __float_as_uint(lane == 0 ? mw : lw));

  float acc[8];
#pragma unroll
  for (int k = 0; k < 8; ++k) acc[k] = 0.f;
#define COMPUTE_V(BUF, j_) do { \
    f32x4 p0_ = *reinterpret_cast<const f32x4*>(sc + (j_) * 32 + r * 8), p1_ = *reinterpret_cast<const f32x4*>(sc + (j_) * 32 + r * 8 + 4); \
    _Pragma("unroll") for (int t_ = 0; t_ < 8; ++t_) { float p_ = t_ < 4 ? p0_[t_ & 3] : p1_[t_ & 3]; \
      _Pragma("unroll") for (int k_ = 0; k_ < 4; ++k_) { \
        acc[2 * k_] = fmaf(p_, lo2f(BUF[t_][k_]), acc[2 * k_]); \
        acc[2 * k_ + 1] = fmaf(p_, hi2f(BUF[t_][k_]), acc[2 * k_ + 1]); } } } while (0)
#pragma unroll 1
  for (int j = 0; j < NT - 2; j += 2) {
    COMPUTE_V(A, j); SB(); ISSUE(A, NT + j + 2); SB();
    COMPUTE_V(Bf, j + 1); SB(); ISSUE(Bf, NT + j + 3); SB();
  }
  COMPUTE_V(A, NT - 2);
  COMPUTE_V(Bf, NT - 1);

  {
    const u64* pp[1] = {ws + WS_ML + (size_t)bh * 64 + lane};
    u32 v[1];
    poll_gran<1>(pp, tag, v);
    float val = __uint_as_float(v[0]);
    float other = dpp<XOR1>(val);
    float mj = (lane & 1) ? other : val, lj = (lane & 1) ? val : other;
    float M = wave_max(mj);
    float L = wave_sum((lane & 1) ? 0.f : __builtin_amdgcn_exp2f(mj - M) * lj);
    float f = __builtin_amdgcn_exp2f(mw - M) / L;
#pragma unroll
    for (int k = 0; k < 8; ++k) acc[k] = xrow_sum(acc[k]) * f;
  }
  if (r == 0) {
    *reinterpret_cast<f32x4*>(lds + L_QP + wave * 128 + 8 * c) = f32x4{acc[0], acc[1], acc[2], acc[3]};
    *reinterpret_cast<f32x4*>(lds + L_QP + wave * 128 + 8 * c + 4) = f32x4{acc[4], acc[5], acc[6], acc[7]};
  }
  __syncthreads();
  {
    float o[16];
#pragma unroll
    for (int k = 0; k < 16; ++k) o[k] = 0.f;
#pragma unroll
    for (int w = 0; w < NW; ++w) {
#pragma unroll
      for (int g = 0; g < 4; ++g) {
        f32x4 a = *reinterpret_cast<const f32x4*>(lds + L_QP + w * 128 + 16 * wave + 4 * g);
#pragma unroll
        for (int k = 0; k < 4; ++k) o[4 * g + k] += a[k];
      }
    }
    float a[8];
#pragma unroll
    for (int k = 0; k < 8; ++k) a[k] = 0.f;
#pragma unroll
    for (int i = 0; i < 16; ++i) {
#pragma unroll
      for (int k = 0; k < 4; ++k) { a[2 * k] = fmaf(o[i], lo2f(W[i][k]), a[2 * k]); a[2 * k + 1] = fmaf(o[i], hi2f(W[i][k]), a[2 * k + 1]); }
    }
    *reinterpret_cast<f32x4*>(sc + 8 * lane) = f32x4{a[0], a[1], a[2], a[3]};
    *reinterpret_cast<f32x4*>(sc + 8 * lane + 4) = f32x4{a[4], a[5], a[6], a[7]};
  }
  __syncthreads();
  {
    float s = 0.f;
#pragma unroll
    for (int w = 0; w < NW; ++w) s += lds[L_SC + w * 512 + tid];
    st_gran(part_p, tag, __float_as_uint(s));
  }

  float* red = lds + L_RED;
  if (role == 1) {
    const int seg = tid >> 1, half = tid & 1;
    const u64* p0 = ws + WS_PART + (size_t)seg * 512 + rcol0 + 4 * half;
    const u64* pp[4] = {p0, p0 + 1, p0 + 2, p0 + 3};
    u32 v[4];
    poll_gran<4>(pp, tag, v);
    float f[4];
#pragma unroll
    for (int k = 0; k < 4; ++k) {
      float t = __uint_as_float(v[k]);
      t += dpp<XOR2>(t); t += dpp<ROR4>(t); t += dpp<ROR8>(t);
      f[k] = xrow_sum(t);
    }
    if (lane < 2) {
#pragma unroll
      for (int k = 0; k < 4; ++k) red[wave * 8 + 4 * lane + k] = f[k];
    }
    __syncthreads();
    if (tid < 8) {
      float s0 = (red[tid] + red[8 + tid]) + (red[16 + tid] + red[24 + tid]);
      float s1 = (red[32 + tid] + red[40 + tid]) + (red[48 + tid] + red[56 + tid]);
      u32 h0 = f2bf(bf2f(x[rcol0 + tid]) + rbf(s0));
      u32 h1 = f2bf(bf2f(x[DM + rcol0 + tid]) + rbf(s1));
      st_gran(ws + WS_HG + rcol0 + tid, tag, h0 | (h1 << 16));
    }
  } else if (role == 2) {
    {
      const u64* pp[1] = {ws + WS_HG + tid};
      u32 v[1];
      poll_gran<1>(pp, tag, v);
      __syncthreads();
      lds[L_SC + tid] = lo2f(v[0]);
      lds[L_SC + 512 + tid] = hi2f(v[0]);
    }
    __syncthreads();
    {
      float* lnb = lds + L_SC + 1024 + wave * 0;
#pragma unroll
      for (int bb = 0; bb < 2; ++bb) {
        float hx[8], ln[8];
        f32x4 a0 = *reinterpret_cast<const f32x4*>(lds + L_SC + bb * 512 + 8 * lane), a1 = *reinterpret_cast<const f32x4*>(lds + L_SC + bb * 512 + 8 * lane + 4);
#pragma unroll
        for (int k = 0; k < 4; ++k) { hx[k] = a0[k]; hx[4 + k] = a1[k]; }
        wave_ln(hx, ln);
        *reinterpret_cast<f32x4*>(lnb + bb * 512 + 8 * lane) = f32x4{ln[0], ln[1], ln[2], ln[3]};
        *reinterpret_cast<f32x4*>(lnb + bb * 512 + 8 * lane + 4) = f32x4{ln[4], ln[5], ln[6], ln[7]};
      }
      const int tt = tid & 255;
      float a0[8], a1[8];
#pragma unroll
      for (int k = 0; k < 8; ++k) { a0[k] = 0.f; a1[k] = 0.f; }
#pragma unroll
      for (int i = 0; i < 4; ++i) {
        float l0 = lnb[(tt >> 1) + 128 * i], l1 = lnb[512 + (tt >> 1) + 128 * i];
#pragma unroll
        for (int k = 0; k < 4; ++k) {
          float wl = lo2f(RW[i][k]), wh = hi2f(RW[i][k]);
          a0[2 * k] = fmaf(l0, wl, a0[2 * k]); a0[2 * k + 1] = fmaf(l0, wh, a0[2 * k + 1]);
          a1[2 * k] = fmaf(l1, wl, a1[2 * k]); a1[2 * k + 1] = fmaf(l1, wh, a1[2 * k + 1]);
        }
      }
#pragma unroll
      for (int k = 0; k < 8; ++k) {
        float t = a0[k]; t += dpp<XOR2>(t); t += dpp<ROR4>(t); t += dpp<ROR8>(t); a0[k] = xrow_sum(t);
        t = a1[k]; t += dpp<XOR2>(t); t += dpp<ROR4>(t); t += dpp<ROR8>(t); a1[k] = xrow_sum(t);
      }
      if (lane < 2) {
#pragma unroll
        for (int k = 0; k < 8; ++k) { red[wave * 32 + 8 * lane + k] = a0[k]; red[wave * 32 + 16 + 8 * lane + k] = a1[k]; }
      }
    }
    __syncthreads();
    if (tid < 16) {
      u32 pk = 0;
#pragma unroll
      for (int bb = 0; bb < 2; ++bb) {
        float g = (red[0 * 32 + bb * 16 + tid] + red[1 * 32 + bb * 16 + tid]) + (red[2 * 32 + bb * 16 + tid] + red[3 * 32 + bb * 16 + tid]);
        float u = (red[4 * 32 + bb * 16 + tid] + red[5 * 32 + bb * 16 + tid]) + (red[6 * 32 + bb * 16 + tid] + red[7 * 32 + bb * 16 + tid]);
        g = rbf(g); u = rbf(u);
        float sg = rbf(g / (1.f + __expf(-g)));
        pk |= f2bf(sg * u) << (16 * bb);
      }
      st_gran(ws + WS_AG + rcol0 + tid, tag, pk);
    }
  } else if (role == 0) {
    const u64* ap = ws + WS_AG + tid;
    const u64* pp[4] = {ap, ap + 512, ap + 1024, tid < 256 ? ap + 1536 : ap};
    u32 v[4];
    poll_gran<4>(pp, tag, v);
    float a0[8], a1[8];
#pragma unroll
    for (int k = 0; k < 8; ++k) { a0[k] = 0.f; a1[k] = 0.f; }
#pragma unroll
    for (int i = 0; i < 4; ++i) {
      float l0 = lo2f(v[i]), l1 = hi2f(v[i]);
      if (i == 3 && tid >= 256) { l0 = 0.f; l1 = 0.f; }
#pragma unroll
      for (int k = 0; k < 4; ++k) {
        float wl = lo2f(RW[i][k]), wh = hi2f(RW[i][k]);
        a0[2 * k] = fmaf(l0, wl, a0[2 * k]); a0[2 * k + 1] = fmaf(l0, wh, a0[2 * k + 1]);
        a1[2 * k] = fmaf(l1, wl, a1[2 * k]); a1[2 * k + 1] = fmaf(l1, wh, a1[2 * k + 1]);
      }
    }
#pragma unroll
    for (int k = 0; k < 8; ++k) { a0[k] = wave_sum(a0[k]); a1[k] = wave_sum(a1[k]); }
    if (lane == 0) {
#pragma unroll
      for (int k = 0; k < 8; ++k) { red[wave * 16 + k] = a0[k]; red[wave * 16 + 8 + k] = a1[k]; }
    }
    __syncthreads();
    if (tid < 16) {
      const u64* pp2[1] = {ws + WS_HG + rcol0 + (tid & 7)};
      u32 hv[1];
      poll_gran<1>(pp2, tag, hv);
      float s = 0.f;
#pragma unroll
      for (int w = 0; w < NW; ++w) s += red[w * 16 + tid];
      float hh = (tid < 8) ? lo2f(hv[0]) : hi2f(hv[0]);
      out[(tid >> 3) * DM + rcol0 + (tid & 7)] = (u16)f2bf(hh + rbf(s));
    }
    if (bid == 0 && tid == 0) __hip_atomic_store((u32*)(ws + WS_E), tag, __ATOMIC_RELAXED, __HIP_MEMORY_SCOPE_AGENT);
  }
}

extern "C" void kernel_launch(void* const* d_in, const int* in_sizes, int n_in,
                              void* d_out, int out_size, void* d_ws, size_t ws_size,
                              hipStream_t stream) {
  fused_kernel<<<256, NTHR, 0, stream>>>((const u16*)d_in[0], (const u16*)d_in[1], (const u16*)d_in[2], (const u16*)d_in[3],
                                         (const u16*)d_in[4], (const u16*)d_in[6], (const u16*)d_in[7], (const u16*)d_in[8],
                                         (u16*)d_out, (u64*)d_ws);
}
```

```cpp
#include <hip/hip_runtime.h>
#include <hip/hip_bf16.h>
#include <stdint.h>

typedef unsigned int u32;
typedef unsigned short u16;
typedef unsigned long long u64;
typedef __attribute__((ext_vector_type(4))) unsigned u32x4;
typedef __attribute__((ext_vector_type(4))) float f32x4;
typedef __attribute__((ext_vector_type(2))) __bf16 bf16x2;

constexpr int NB = 2, DM = 512, NH = 32, SK = 16384, DH = 128, FF = 1792;
constexpr int NSPLIT = 4;
constexpr int NW = 8;
constexpr int NTHR = NW * 64;
constexpr int KPW = SK / NSPLIT / NW;
constexpr int TK = 32;
constexpr int NT = KPW / TK;
constexpr float SCALE = 0.08838834764831845f;
constexpr float LOG2E = 1.4426950408889634f;
constexpr unsigned SPIN_CAP = 1u << 22;

constexpr size_t WS_E = 0;
constexpr size_t WS_ML = 64;
constexpr size_t WS_PART = WS_ML + 64 * 64;
constexpr size_t WS_HG = WS_PART + 2 * 128 * 512;
constexpr size_t WS_AG = WS_HG + 512;

__device__ __forceinline__ float bf2f(u32 v) { return __uint_as_float(v << 16); }
__device__ __forceinline__ float lo2f(u32 v) { return __uint_as_float(v << 16); }
__device__ __forceinline__ float hi2f(u32 v) { return __uint_as_float(v & 0xffff0000u); }
__device__ __forceinline__ float rbf(float x) { return (float)(__bf16)x; }
__device__ __forceinline__ u32 f2bf(float x) { __bf16 b = (__bf16)x; return (u32)__builtin_bit_cast(u16, b); }
__device__ __forceinline__ float dot2(u32 a, u32 b, float acc) {
  return __builtin_amdgcn_fdot2_f32_bf16(__builtin_bit_cast(bf16x2, a), __builtin_bit_cast(bf16x2, b), acc, false);
}
template <int CTRL> __device__ __forceinline__ float dpp(float x) {
  return __builtin_bit_cast(float, __builtin_amdgcn_mov_dpp(__builtin_bit_cast(int, x), CTRL, 0xf, 0xf, true));
}
constexpr int XOR1 = 0xB1, XOR2 = 0x4E, XOR7 = 0x141, ROR8 = 0x128, ROR4 = 0x124, ROR2 = 0x122, ROR1 = 0x121;
__device__ __forceinline__ float xrow_sum(float x) {
  auto s = __builtin_amdgcn_permlane16_swap(__float_as_uint(x), __float_as_uint(x), false, false);
  x = __uint_as_float(s[0]) + __uint_as_float(s[1]);
  auto t = __builtin_amdgcn_permlane32_swap(__float_as_uint(x), __float_as_uint(x), false, false);
  return __uint_as_float(t[0]) + __uint_as_float(t[1]);
}
__device__ __forceinline__ float xrow_max(float x) {
  auto s = __builtin_amdgcn_permlane16_swap(__float_as_uint(x), __float_as_uint(x), false, false);
  x = fmaxf(__uint_as_float(s[0]), __uint_as_float(s[1]));
  auto t = __builtin_amdgcn_permlane32_swap(__float_as_uint(x), __float_as_uint(x), false, false);
  return fmaxf(__uint_as_float(t[0]), __uint_as_float(t[1]));
}
__device__ __forceinline__ float wave_sum(float x) {
  x += dpp<ROR8>(x); x += dpp<ROR4>(x); x += dpp<ROR2>(x); x += dpp<ROR1>(x);
  return xrow_sum(x);
}
__device__ __forceinline__ float wave_max(float x) {
  x = fmaxf(x, dpp<ROR8>(x)); x = fmaxf(x, dpp<ROR4>(x)); x = fmaxf(x, dpp<ROR2>(x)); x = fmaxf(x, dpp<ROR1>(x));
  return xrow_max(x);
}
__device__ __forceinline__ void st_gran(u64* p, u32 tag, u32 val) {
  __hip_atomic_store(p, ((u64)tag << 32) | (u64)val, __ATOMIC_RELAXED, __HIP_MEMORY_SCOPE_AGENT);
}
__device__ __forceinline__ u64 ld_gran(const u64* p) {
  return __hip_atomic_load(p, __ATOMIC_RELAXED, __HIP_MEMORY_SCOPE_AGENT);
}
template <int N> __device__ __forceinline__ void poll_gran(const u64* const (&p)[N], u32 tag, u32 (&v)[N]) {
  for (unsigned spins = 0;; ++spins) {
    bool ok = true;
#pragma unroll
    for (int k = 0; k < N; ++k) { u64 g = ld_gran(p[k]); v[k] = (u32)g; ok &= ((u32)(g >> 32) == tag); }
    if (__all(ok) || spins > SPIN_CAP) break;
    __builtin_amdgcn_s_sleep(1);
  }
}

__device__ __forceinline__ void wave_ln(const float (&x)[8], float (&y)[8]) {
  float s = ((x[0] + x[1]) + (x[2] + x[3])) + ((x[4] + x[5]) + (x[6] + x[7]));
  float m = wave_sum(s) * (1.f / 512.f);
  float v = 0.f;
#pragma unroll
  for (int k = 0; k < 8; ++k) v += (x[k] - m) * (x[k] - m);
  v = wave_sum(v) * (1.f / 512.f);
  m = rbf(m); v = rbf(v);
  float d = rbf(sqrtf(rbf(v + 1e-5f)));
#pragma unroll
  for (int k = 0; k < 8; ++k) y[k] = rbf(rbf(x[k] - m) / d);
}

#define SB() __builtin_amdgcn_sched_barrier(0)
#define STAMP(i_) do {} while (0)
constexpr int L_SC = 0;
constexpr int L_QP = L_SC + NW * 512;
constexpr int L_RED = L_QP + NW * 128;
constexpr int L_TOT = L_RED + 256;

__global__ void __launch_bounds__(NTHR, 2) fused_kernel(const u16* __restrict__ x, const u16* __restrict__ kc, const u16* __restrict__ vc,
    const u16* __restrict__ wq, const u16* __restrict__ wo, const u16* __restrict__ wg, const u16* __restrict__ wu,
    const u16* __restrict__ wd, u16* __restrict__ out, u64* __restrict__ ws) {
  __shared__ __attribute__((aligned(16))) float lds[L_TOT];
  const int tid = threadIdx.x, lane = tid & 63, wave = __builtin_amdgcn_readfirstlane(tid >> 6), r = lane >> 4, c = lane & 15;
  const int bid = blockIdx.x, xcd = bid & 7, idx = bid >> 3;
  const int head = xcd * 4 + (idx & 3), b = (idx >> 2) & 1, split = idx >> 3;
  const int bh = b * NH + head;
  const int role = idx < 8 ? 0 : (idx < 16 ? 1 : (idx < 30 ? 2 : 3));

  STAMP(0);
  const u32 epoch = (u32)__hip_atomic_load((const u32*)(ws + WS_E), __ATOMIC_RELAXED, __HIP_MEMORY_SCOPE_AGENT);
  u32x4 xv = *reinterpret_cast<const u32x4*>(x + b * DM + 8 * lane);
  SB();
  u32x4 W[16];
  {
    const u16* p = wq + (size_t)(64 * wave + r) * 4096 + head * DH + 8 * c;
#pragma unroll
    for (int i = 0; i < 16; ++i) W[i] = *reinterpret_cast<const u32x4*>(p + (size_t)i * 4 * 4096);
  }

  const size_t base = ((size_t)bh * SK + (size_t)split * (SK / NSPLIT) + (size_t)wave * TK) * DH + (size_t)lane * 8;
  const u16* kb = kc + base;
  const u16* vb = vc + base;
  u32x4 A[8], Bf[8];
#define ISSUE(BUF, j_) do { const u16* s_ = ((j_) < NT ? kb : vb) + (size_t)((j_) & (NT - 1)) * (NW * TK * DH); \
    _Pragma("unroll") for (int t_ = 0; t_ < 8; ++t_) BUF[t_] = __builtin_nontemporal_load(reinterpret_cast<const u32x4*>(s_ + t_ * 4 * DH)); } while (0)
  SB();
  ISSUE(A, 0);
  ISSUE(Bf, 1);
  SB();

  float* sc = lds + L_SC + wave * 512;
  {
    float xf[8], ln[8];
#pragma unroll
    for (int k = 0; k < 4; ++k) { xf[2 * k] = lo2f(xv[k]); xf[2 * k + 1] = hi2f(xv[k]); }
    wave_ln(xf, ln);
    *reinterpret_cast<f32x4*>(sc + 8 * lane) = f32x4{ln[0], ln[1], ln[2], ln[3]};
    *reinterpret_cast<f32x4*>(sc + 8 * lane + 4) = f32x4{ln[4], ln[5], ln[6], ln[7]};
  }
  float qa[8];
#pragma unroll
  for (int k = 0; k < 8; ++k) qa[k] = 0.f;
#pragma unroll
  for (int i = 0; i < 16; ++i) {
    float l = sc[64 * wave + 4 * i + r];
#pragma unroll
    for (int k = 0; k < 4; ++k) { qa[2 * k] = fmaf(l, lo2f(W[i][k]), qa[2 * k]); qa[2 * k + 1] = fmaf(l, hi2f(W[i][k]), qa[2 * k + 1]); }
  }
#pragma unroll
  for (int k = 0; k < 8; ++k) qa[k] = xrow_sum(qa[k]);
  if (r == 0) {
    *reinterpret_cast<f32x4*>(lds + L_QP + wave * 128 + 8 * c) = f32x4{qa[0], qa[1], qa[2], qa[3]};
    *reinterpret_cast<f32x4*>(lds + L_QP + wave * 128 + 8 * c + 4) = f32x4{qa[4], qa[5], qa[6], qa[7]};
  }
  {
    const u16* p = wo + (size_t)(head * DH + 16 * wave) * DM + 8 * lane;
#pragma unroll
    for (int i = 0; i < 16; ++i) W[i] = *reinterpret_cast<const u32x4*>(p + (size_t)i * DM);
  }
  u32x4 RW[4];
  int rcol0;
  {
    const u16* p; size_t st;
    if (role == 0) {
      rcol0 = 8 * (xcd * 8 + idx);
      p = wd + (size_t)tid * DM + rcol0; st = (size_t)512 * DM;
    } else if (role == 2) {
      rcol0 = 16 * (xcd * 14 + idx - 16);
      const int tt = tid & 255;
      p = (wave < 4 ? wg : wu) + (size_t)(tt >> 1) * FF + rcol0 + 8 * (tt & 1); st = (size_t)128 * FF;
    } else {
      rcol0 = 8 * (xcd * 8 + ((idx - 8) & 7));
      p = wd; st = 0;
    }
#pragma unroll
    for (int i = 0; i < 3; ++i) RW[i] = *reinterpret_cast<const u32x4*>(p + (size_t)i * st);
    RW[3] = *reinterpret_cast<const u32x4*>(p + ((role == 0 && tid >= 256) ? (size_t)0 : 3 * st));
  }
  const u32 tag = epoch + 1u;
  u64* part_p = ws + WS_PART + ((size_t)b * 128 + head * 4 + split) * 512 + tid;
  u64* ml_p = ws + WS_ML + (size_t)bh * 64 + (split * NW + wave) * 2;
  st_gran(part_p, epoch, 0u);
  if (lane < 2) st_gran(ml_p + lane, epoch, 0u);
  if (role == 1 && tid < 8) st_gran(ws + WS_HG + rcol0 + tid, epoch, 0u);
  if (role == 2 && tid < 16) st_gran(ws + WS_AG + rcol0 + tid, epoch, 0u);

  __syncthreads();
  u32 qp[4];
  {
    float q[8];
#pragma unroll
    for (int k = 0; k < 8; ++k) q[k] = 0.f;
#pragma unroll
    for (int w = 0; w < NW; ++w) {
      f32x4 a0 = *reinterpret_cast<const f32x4*>(lds + L_QP + w * 128 + 8 * c);
      f32x4 a1 = *reinterpret_cast<const f32x4*>(lds + L_QP + w * 128 + 8 * c + 4);
#pragma unroll
      for (int k = 0; k < 4; ++k) { q[k] += a0[k]; q[4 + k] += a1[k]; }
    }
#pragma unroll
    for (int k = 0; k < 4; ++k) qp[k] = f2bf(q[2 * k]) | (f2bf(q[2 * k + 1]) << 16);
  }

  STAMP(1);
  float mx = -INFINITY;
#define COMPUTE_K(BUF, j_) do { float x_[8]; \
    _Pragma("unroll") for (int t_ = 0; t_ < 8; ++t_) { float d_ = dot2(BUF[t_][0], qp[0], 0.f); d_ = dot2(BUF[t_][1], qp[1], d_); \
      d_ = dot2(BUF[t_][2], qp[2], d_); x_[t_] = dot2(BUF[t_][3], qp[3], d_); } \
    _Pragma("unroll") for (int h_ = 0; h_ < 4; ++h_) { float t1_ = x_[h_] + dpp<ROR8>(x_[h_]), t2_ = x_[h_ + 4] + dpp<ROR8>(x_[h_ + 4]); x_[h_] = (c & 8) ? t2_ : t1_; } \
    _Pragma("unroll") for (int h_ = 0; h_ < 2; ++h_) { float t1_ = x_[h_] + dpp<XOR7>(x_[h_]), t2_ = x_[h_ + 2] + dpp<XOR7>(x_[h_ + 2]); x_[h_] = (c & 4) ? t2_ : t1_; } \
    { float t1_ = x_[0] + dpp<XOR2>(x_[0]), t2_ = x_[1] + dpp<XOR2>(x_[1]); x_[0] = (c & 2) ? t2_ : t1_; } \
    x_[0] += dpp<XOR1>(x_[0]); \
    float s_ = rbf(rbf(x_[0]) * SCALE) * LOG2E; \
    mx = fmaxf(mx, s_); \
    sc[(j_) * 32 + r * 8 + (c >> 1)] = s_; } while (0)

  SB();
  COMPUTE_K(A, 0); SB(); ISSUE(A, 2); SB();
  COMPUTE_K(Bf, 1); SB(); ISSUE(Bf, 3); SB();
#pragma unroll 1
  for (int j = 2; j < NT; j += 2) {
    COMPUTE_K(A, j); SB(); ISSUE(A, j + 2); SB();
    COMPUTE_K(Bf, j + 1); SB(); ISSUE(Bf, j + 3); SB();
  }
  STAMP(2);
  const float mw = wave_max(mx);
  float lw;
  {
    f32x4 s0 = *reinterpret_cast<const f32x4*>(sc + 8 * lane), s1 = *reinterpret_cast<const f32x4*>(sc + 8 * lane + 4);
    float l = 0.f;
#pragma unroll
    for (int k = 0; k < 4; ++k) { s0[k] = __builtin_amdgcn_exp2f(s0[k] - mw); s1[k] = __builtin_amdgcn_exp2f(s1[k] - mw); l += s0[k] + s1[k]; }
    *reinterpret_cast<f32x4*>(sc + 8 * lane) = s0;
    *reinterpret_cast<f32x4*>(sc + 8 * lane + 4) = s1;
    lw = wave_sum(l);
  }
  if (lane < 2) st_gran(ml_p + lane, tag, __float_as_uint(lane == 0 ? mw : lw));

  STAMP(3);
  float acc[8];
#pragma unroll
  for (int k = 0; k < 8; ++k) acc[k] = 0.f;
#define COMPUTE_V(BUF, j_) do { \
    f32x4 p0_ = *reinterpret_cast<const f32x4*>(sc + (j_) * 32 + r * 8), p1_ = *reinterpret_cast<const f32x4*>(sc + (j_) * 32 + r * 8 + 4); \
    _Pragma("unroll") for (int t_ = 0; t_ < 8; ++t_) { float p_ = t_ < 4 ? p0_[t_ & 3] : p1_[t_ & 3]; \
      _Pragma("unroll") for (int k_ = 0; k_ < 4; ++k_) { \
        acc[2 * k_] = fmaf(p_, lo2f(BUF[t_][k_]), acc[2 * k_]); \
        acc[2 * k_ + 1] = fmaf(p_, hi2f(BUF[t_][k_]), acc[2 * k_ + 1]); } } } while (0)
#pragma unroll 1
  for (int j = 0; j < NT - 2; j += 2) {
    COMPUTE_V(A, j); SB(); ISSUE(A, NT + j + 2); SB();
    COMPUTE_V(Bf, j + 1); SB(); ISSUE(Bf, NT + j + 3); SB();
  }
  COMPUTE_V(A, NT - 2);
  COMPUTE_V(Bf, NT - 1);

  STAMP(4);
  {
    const u64* pp[1] = {ws + WS_ML + (size_t)bh * 64 + lane};
    u32 v[1];
    poll_gran<1>(pp, tag, v);
    float val = __uint_as_float(v[0]);
    float other = dpp<XOR1>(val);
    float mj = (lane & 1) ? other : val, lj = (lane & 1) ? val : other;
    float M = wave_max(mj);
    float L = wave_sum((lane & 1) ? 0.f : __builtin_amdgcn_exp2f(mj - M) * lj);
    float f = __builtin_amdgcn_exp2f(mw - M) / L;
#pragma unroll
    for (int k = 0; k < 8; ++k) acc[k] = xrow_sum(acc[k]) * f;
  }
  if (r == 0) {
    *reinterpret_cast<f32x4*>(lds + L_QP + wave * 128 + 8 * c) = f32x4{acc[0], acc[1], acc[2], acc[3]};
    *reinterpret_cast<f32x4*>(lds + L_QP + wave * 128 + 8 * c + 4) = f32x4{acc[4], acc[5], acc[6], acc[7]};
  }
  __syncthreads();
  STAMP(5);
  {
    float o[16];
#pragma unroll
    for (int k = 0; k < 16; ++k) o[k] = 0.f;
#pragma unroll
    for (int w = 0; w < NW; ++w) {
#pragma unroll
      for (int g = 0; g < 4; ++g) {
        f32x4 a = *reinterpret_cast<const f32x4*>(lds + L_QP + w * 128 + 16 * wave + 4 * g);
#pragma unroll
        for (int k = 0; k < 4; ++k) o[4 * g + k] += a[k];
      }
    }
    float a[8];
#pragma unroll
    for (int k = 0; k < 8; ++k) a[k] = 0.f;
#pragma unroll
    for (int i = 0; i < 16; ++i) {
#pragma unroll
      for (int k = 0; k < 4; ++k) { a[2 * k] = fmaf(o[i], lo2f(W[i][k]), a[2 * k]); a[2 * k + 1] = fmaf(o[i], hi2f(W[i][k]), a[2 * k + 1]); }
    }
    *reinterpret_cast<f32x4*>(sc + 8 * lane) = f32x4{a[0], a[1], a[2], a[3]};
    *reinterpret_cast<f32x4*>(sc + 8 * lane + 4) = f32x4{a[4], a[5], a[6], a[7]};
  }
  __syncthreads();
  {
    float s = 0.f;
#pragma unroll
    for (int w = 0; w < NW; ++w) s += lds[L_SC + w * 512 + tid];
    st_gran(part_p, tag, __float_as_uint(s));
  }

  STAMP(6);
  float* red = lds + L_RED;
  if (role == 1) {
    const int seg = tid >> 1, half = tid & 1;
    const u64* p0 = ws + WS_PART + (size_t)seg * 512 + rcol0 + 4 * half;
    const u64* pp[4] = {p0, p0 + 1, p0 + 2, p0 + 3};
    u32 v[4];
    poll_gran<4>(pp, tag, v);
    STAMP(7);
    float f[4];
#pragma unroll
    for (int k = 0; k < 4; ++k) {
      float t = __uint_as_float(v[k]);
      t += dpp<XOR2>(t); t += dpp<ROR4>(t); t += dpp<ROR8>(t);
      f[k] = xrow_sum(t);
    }
    if (lane < 2) {
#pragma unroll
      for (int k = 0; k < 4; ++k) red[wave * 8 + 4 * lane + k] = f[k];
    }
    __syncthreads();
    if (tid < 8) {
      float s0 = (red[tid] + red[8 + tid]) + (red[16 + tid] + red[24 + tid]);
      float s1 = (red[32 + tid] + red[40 + tid]) + (red[48 + tid] + red[56 + tid]);
      u32 h0 = f2bf(bf2f(x[rcol0 + tid]) + rbf(s0));
      u32 h1 = f2bf(bf2f(x[DM + rcol0 + tid]) + rbf(s1));
      st_gran(ws + WS_HG + rcol0 + tid, tag, h0 | (h1 << 16));
    }
    STAMP(8);
  } else if (role == 2) {
    {
      const u64* pp[1] = {ws + WS_HG + tid};
      u32 v[1];
      poll_gran<1>(pp, tag, v);
      STAMP(7);
      __syncthreads();
      lds[L_SC + tid] = lo2f(v[0]);
      lds[L_SC + 512 + tid] = hi2f(v[0]);
    }
    __syncthreads();
    {
      float* lnb = lds + L_SC + 1024 + wave * 0;
#pragma unroll
      for (int bb = 0; bb < 2; ++bb) {
        float hx[8], ln[8];
        f32x4 a0 = *reinterpret_cast<const f32x4*>(lds + L_SC + bb * 512 + 8 * lane), a1 = *reinterpret_cast<const f32x4*>(lds + L_SC + bb * 512 + 8 * lane + 4);
#pragma unroll
        for (int k = 0; k < 4; ++k) { hx[k] = a0[k]; hx[4 + k] = a1[k]; }
        wave_ln(hx, ln);
        *reinterpret_cast<f32x4*>(lnb + bb * 512 + 8 * lane) = f32x4{ln[0], ln[1], ln[2], ln[3]};
        *reinterpret_cast<f32x4*>(lnb + bb * 512 + 8 * lane + 4) = f32x4{ln[4], ln[5], ln[6], ln[7]};
      }
      const int tt = tid & 255;
      float a0[8], a1[8];
#pragma unroll
      for (int k = 0; k < 8; ++k) { a0[k] = 0.f; a1[k] = 0.f; }
#pragma unroll
      for (int i = 0; i < 4; ++i) {
        float l0 = lnb[(tt >> 1) + 128 * i], l1 = lnb[512 + (tt >> 1) + 128 * i];
#pragma unroll
        for (int k = 0; k < 4; ++k) {
          float wl = lo2f(RW[i][k]), wh = hi2f(RW[i][k]);
          a0[2 * k] = fmaf(l0, wl, a0[2 * k]); a0[2 * k + 1] = fmaf(l0, wh, a0[2 * k + 1]);
          a1[2 * k] = fmaf(l1, wl, a1[2 * k]); a1[2 * k + 1] = fmaf(l1, wh, a1[2 * k + 1]);
        }
      }
#pragma unroll
      for (int k = 0; k < 8; ++k) {
        float t = a0[k]; t += dpp<XOR2>(t); t += dpp<ROR4>(t); t += dpp<ROR8>(t); a0[k] = xrow_sum(t);
        t = a1[k]; t += dpp<XOR2>(t); t += dpp<ROR4>(t); t += dpp<ROR8>(t); a1[k] = xrow_sum(t);
      }
      if (lane < 2) {
#pragma unroll
        for (int k = 0; k < 8; ++k) { red[wave * 32 + 8 * lane + k] = a0[k]; red[wave * 32 + 16 + 8 * lane + k] = a1[k]; }
      }
    }
    __syncthreads();
    if (tid < 16) {
      u32 pk = 0;
#pragma unroll
      for (int bb = 0; bb < 2; ++bb) {
        float g = (red[0 * 32 + bb * 16 + tid] + red[1 * 32 + bb * 16 + tid]) + (red[2 * 32 + bb * 16 + tid] + red[3 * 32 + bb * 16 + tid]);
        float u = (red[4 * 32 + bb * 16 + tid] + red[5 * 32 + bb * 16 + tid]) + (red[6 * 32 + bb * 16 + tid] + red[7 * 32 + bb * 16 + tid]);
        g = rbf(g); u = rbf(u);
        float sg = rbf(g / (1.f + __expf(-g)));
        pk |= f2bf(sg * u) << (16 * bb);
      }
      st_gran(ws + WS_AG + rcol0 + tid, tag, pk);
    }
    STAMP(8);
  } else if (role == 0) {
    const u64* ap = ws + WS_AG + tid;
    const u64* pp[4] = {ap, ap + 512, ap + 1024, tid < 256 ? ap + 1536 : ap};
    u32 v[4];
    poll_gran<4>(pp, tag, v);
    STAMP(7);
    float a0[8], a1[8];
#pragma unroll
    for (int k = 0; k < 8; ++k) { a0[k] = 0.f; a1[k] = 0.f; }
#pragma unroll
    for (int i = 0; i < 4; ++i) {
      float l0 = lo2f(v[i]), l1 = hi2f(v[i]);
      if (i == 3 && tid >= 256) { l0 = 0.f; l1 = 0.f; }
#pragma unroll
      for (int k = 0; k < 4; ++k) {
        float wl = lo2f(RW[i][k]), wh = hi2f(RW[i][k]);
        a0[2 * k] = fmaf(l0, wl, a0[2 * k]); a0[2 * k + 1] = fmaf(l0, wh, a0[2 * k + 1]);
        a1[2 * k] = fmaf(l1, wl, a1[2 * k]); a1[2 * k + 1] = fmaf(l1, wh, a1[2 * k + 1]);
      }
    }
#pragma unroll
    for (int k = 0; k < 8; ++k) { a0[k] = wave_sum(a0[k]); a1[k] = wave_sum(a1[k]); }
    if (lane == 0) {
#pragma unroll
      for (int k = 0; k < 8; ++k) { red[wave * 16 + k] = a0[k]; red[wave * 16 + 8 + k] = a1[k]; }
    }
    __syncthreads();
    if (tid < 16) {
      const u64* pp2[1] = {ws + WS_HG + rcol0 + (tid & 7)};
      u32 hv[1];
      poll_gran<1>(pp2, tag, hv);
      float s = 0.f;
#pragma unroll
      for (int w = 0; w < NW; ++w) s += red[w * 16 + tid];
      float hh = (tid < 8) ? lo2f(hv[0]) : hi2f(hv[0]);
      out[(tid >> 3) * DM + rcol0 + (tid & 7)] = (u16)f2bf(hh + rbf(s));
    }
    STAMP(8);
    if (bid == 0 && tid == 0) __hip_atomic_store((u32*)(ws + WS_E), tag, __ATOMIC_RELAXED, __HIP_MEMORY_SCOPE_AGENT);
  }
}

extern "C" void kernel_launch(void* const* d_in, const int* in_sizes, int n_in,
                              void* d_out, int out_size, void* d_ws, size_t ws_size,
                              hipStream_t stream) {
  fused_kernel<<<256, NTHR, 0, stream>>>((const u16*)d_in[0], (const u16*)d_in[1], (const u16*)d_in[2], (const u16*)d_in[3],
                                         (const u16*)d_in[4], (const u16*)d_in[6], (const u16*)d_in[7], (const u16*)d_in[8],
                                         (u16*)d_out, (u64*)d_ws);
}
```

```cpp
#include <hip/hip_runtime.h>
#include <hip/hip_bf16.h>
#include <stdint.h>

typedef unsigned int u32;
typedef unsigned short u16;
typedef unsigned long long u64;
typedef __attribute__((ext_vector_type(4))) unsigned u32x4;
typedef __attribute__((ext_vector_type(4))) float f32x4;
typedef __attribute__((ext_vector_type(2))) float f32x2;
typedef __attribute__((ext_vector_type(2))) __bf16 bf16x2;

constexpr int NB = 2, DM = 512, NH = 32, SK = 16384, DH = 128, FF = 1792;
constexpr int NSPLIT = 4;
constexpr int NW = 8;
constexpr int NTHR = NW * 64;
constexpr int KPW = SK / NSPLIT / NW;
constexpr int TK = 32;
constexpr int NT = KPW / TK;
constexpr float SCALE = 0.08838834764831845f;
constexpr float LOG2E = 1.4426950408889634f;
constexpr unsigned SPIN_CAP = 1u << 22;

constexpr size_t WS_E = 0;
constexpr size_t WS_QG = 64;
constexpr size_t WS_ML = WS_QG + 64 * 64;
constexpr size_t WS_PART = WS_ML + 64 * 64;
constexpr size_t WS_HG = WS_PART + 2 * 128 * 512;
constexpr size_t WS_AG = WS_HG + 512;

__device__ __forceinline__ float bf2f(u32 v) { return __uint_as_float(v << 16); }
__device__ __forceinline__ float lo2f(u32 v) { return __uint_as_float(v << 16); }
__device__ __forceinline__ float hi2f(u32 v) { return __uint_as_float(v & 0xffff0000u); }
__device__ __forceinline__ float rbf(float x) { return (float)(__bf16)x; }
__device__ __forceinline__ u32 f2bf(float x) { __bf16 b = (__bf16)x; return (u32)__builtin_bit_cast(u16, b); }
__device__ __forceinline__ float dot2(u32 a, u32 b, float acc) {
  return __builtin_amdgcn_fdot2_f32_bf16(__builtin_bit_cast(bf16x2, a), __builtin_bit_cast(bf16x2, b), acc, false);
}
template <int CTRL> __device__ __forceinline__ float dpp(float x) {
  return __builtin_bit_cast(float, __builtin_amdgcn_mov_dpp(__builtin_bit_cast(int, x), CTRL, 0xf, 0xf, true));
}
constexpr int XOR1 = 0xB1, XOR2 = 0x4E, XOR7 = 0x141, ROR8 = 0x128, ROR4 = 0x124, ROR2 = 0x122, ROR1 = 0x121;
__device__ __forceinline__ float red32(float a, float b) {
  auto t = __builtin_amdgcn_permlane32_swap(__float_as_uint(a), __float_as_uint(b), false, false);
  return __uint_as_float(t[0]) + __uint_as_float(t[1]);
}
__device__ __forceinline__ float red16(float a, float b) {
  auto t = __builtin_amdgcn_permlane16_swap(__float_as_uint(a), __float_as_uint(b), false, false);
  return __uint_as_float(t[0]) + __uint_as_float(t[1]);
}
template <int CTRL> __device__ __forceinline__ float redd(float a, float b, bool hi) {
  const float t1 = a + dpp<CTRL>(a), t2 = b + dpp<CTRL>(b);
  return hi ? t2 : t1;
}
__device__ __forceinline__ float xrow_sum(float x) { x = red16(x, x); return red32(x, x); }
__device__ __forceinline__ float xrow_max(float x) {
  auto s = __builtin_amdgcn_permlane16_swap(__float_as_uint(x), __float_as_uint(x), false, false);
  x = fmaxf(__uint_as_float(s[0]), __uint_as_float(s[1]));
  auto t = __builtin_amdgcn_permlane32_swap(__float_as_uint(x), __float_as_uint(x), false, false);
  return fmaxf(__uint_as_float(t[0]), __uint_as_float(t[1]));
}
__device__ __forceinline__ float wave_sum(float x) {
  x += dpp<ROR8>(x); x += dpp<ROR4>(x); x += dpp<ROR2>(x); x += dpp<ROR1>(x);
  return xrow_sum(x);
}
__device__ __forceinline__ float wave_max(float x) {
  x = fmaxf(x, dpp<ROR8>(x)); x = fmaxf(x, dpp<ROR4>(x)); x = fmaxf(x, dpp<ROR2>(x)); x = fmaxf(x, dpp<ROR1>(x));
  return xrow_max(x);
}
__device__ __forceinline__ void st_gran(u64* p, u32 tag, u32 val) {
  __hip_atomic_store(p, ((u64)tag << 32) | (u64)val, __ATOMIC_RELAXED, __HIP_MEMORY_SCOPE_AGENT);
}
__device__ __forceinline__ u64 ld_gran(const u64* p) {
  return __hip_atomic_load(p, __ATOMIC_RELAXED, __HIP_MEMORY_SCOPE_AGENT);
}
template <int N> __device__ __forceinline__ void poll_gran(const u64* const (&p)[N], u32 tag, u32 (&v)[N]) {
  for (unsigned spins = 0;; ++spins) {
    bool ok = true;
#pragma unroll
    for (int k = 0; k < N; ++k) { u64 g = ld_gran(p[k]); v[k] = (u32)g; ok &= ((u32)(g >> 32) == tag); }
    if (__all(ok) || spins > SPIN_CAP) break;
    __builtin_amdgcn_s_sleep(1);
  }
}
__device__ __forceinline__ void fma8(f32x2 (&a)[4], float s, u32x4 w) {
  const f32x2 ss = {s, s};
#pragma unroll
  for (int k = 0; k < 4; ++k) { const f32x2 v = {lo2f(w[k]), hi2f(w[k])}; a[k] = __builtin_elementwise_fma(ss, v, a[k]); }
  asm volatile("" : "+v"(a[0]), "+v"(a[1]), "+v"(a[2]), "+v"(a[3]));
}
__device__ __forceinline__ void wave_ln_stats2(const float (&x)[8], const float (&y)[8], float& m0, float& d0, float& m1, float& d1) {
  float s0 = ((x[0] + x[1]) + (x[2] + x[3])) + ((x[4] + x[5]) + (x[6] + x[7]));
  float s1 = ((y[0] + y[1]) + (y[2] + y[3])) + ((y[4] + y[5]) + (y[6] + y[7]));
  s0 = wave_sum(s0) * (1.f / 512.f); s1 = wave_sum(s1) * (1.f / 512.f);
  float v0 = 0.f, v1 = 0.f;
#pragma unroll
  for (int k = 0; k < 8; ++k) { v0 += (x[k] - s0) * (x[k] - s0); v1 += (y[k] - s1) * (y[k] - s1); }
  v0 = wave_sum(v0) * (1.f / 512.f); v1 = wave_sum(v1) * (1.f / 512.f);
  m0 = rbf(s0); m1 = rbf(s1);
  d0 = rbf(sqrtf(rbf(rbf(v0) + 1e-5f))); d1 = rbf(sqrtf(rbf(rbf(v1) + 1e-5f)));
}

#define SB() __builtin_amdgcn_sched_barrier(0)
#define LAS __attribute__((address_space(3)))
#define FENCE() do { asm volatile("" ::: "memory"); __builtin_amdgcn_sched_barrier(0); } while (0)
__device__ __forceinline__ void glds16(const void* gsrc, unsigned lds_dst) {
  unsigned keep;
  asm volatile("s_mov_b32 %0, m0\n\ts_mov_b32 m0, %2\n\ts_nop 0\n\tglobal_load_lds_dwordx4 %1, off\n\ts_mov_b32 m0, %0" : "=&s"(keep) : "v"(gsrc), "s"(lds_dst) : "memory");
}
#define STAMP(i_) do {} while (0)
constexpr int L_SC = 0;
constexpr int L_QP = L_SC + NW * 512;
constexpr int L_RED = L_QP + NW * 128;
constexpr int L_WO = L_RED + 256;
constexpr int L_TOT = L_WO + NW * 16 * 256;

__global__ void __launch_bounds__(NTHR, 2) fused_kernel(const u16* __restrict__ x, const u16* __restrict__ kc, const u16* __restrict__ vc,
    const u16* __restrict__ wq, const u16* __restrict__ wo, const u16* __restrict__ wg, const u16* __restrict__ wu,
    const u16* __restrict__ wd, u16* __restrict__ out, u64* __restrict__ ws) {
  __shared__ __attribute__((aligned(16))) float lds[L_TOT];
  const int tid = threadIdx.x, lane = tid & 63, r = lane >> 4, c = lane & 15;
  const int wave = __builtin_amdgcn_readfirstlane(tid >> 6);
  const int bid = blockIdx.x, xcd = bid & 7, idx = bid >> 3;
  const int head = xcd * 4 + (idx & 3), g8 = idx >> 2, b = g8 & 1, split = g8 >> 1;
  const int bh = b * NH + head;
  const int role = idx < 8 ? 0 : (idx < 16 ? 1 : (idx < 30 ? 2 : 3));
  const int rcol0 = role == 0 ? 8 * (xcd * 8 + idx) : (role == 2 ? 16 * (xcd * 14 + idx - 16) : 8 * (xcd * 8 + ((idx - 8) & 7)));
  float* const sc = lds + L_SC + wave * 512;
  float* const red = lds + L_RED;
  u32x4* const wol = reinterpret_cast<u32x4*>(lds + L_WO) + wave * 16 * 64 + lane;

  STAMP(0);
  const u32 epoch = (u32)__hip_atomic_load((const u32*)(ws + WS_E), __ATOMIC_RELAXED, __HIP_MEMORY_SCOPE_AGENT);
  const u32x4 xa = *reinterpret_cast<const u32x4*>(x + 8 * lane), xb = *reinterpret_cast<const u32x4*>(x + DM + 8 * lane);
  const u32 xo0 = x[tid], xo1 = x[DM + tid];
  const u32x4 wq0 = *reinterpret_cast<const u32x4*>(wq + (size_t)tid * 4096 + head * DH + 16 * g8);
  const u32x4 wq1 = *reinterpret_cast<const u32x4*>(wq + (size_t)tid * 4096 + head * DH + 16 * g8 + 8);
  const size_t base = ((size_t)bh * SK + (size_t)split * (SK / NSPLIT) + (size_t)wave * TK) * DH + (size_t)lane * 8;
  const u16* kb = kc + base;
  const u16* vb = vc + base;
  u32x4 A[8], Bf[8];
#define ISSUE(BUF, j_) do { const u16* s_ = ((j_) < NT ? kb : vb) + (size_t)((j_) & (NT - 1)) * (NW * TK * DH); \
    _Pragma("unroll") for (int t_ = 0; t_ < 8; ++t_) BUF[t_] = __builtin_nontemporal_load(reinterpret_cast<const u32x4*>(s_ + t_ * 4 * DH)); } while (0)
  SB();
  __builtin_amdgcn_s_barrier();
  SB();
  ISSUE(A, 0);
  SB();

  const u32 tag = epoch + 1u;
  {
    float xf[8], yf[8], m0, d0, m1, d1;
#pragma unroll
    for (int k = 0; k < 4; ++k) { xf[2 * k] = lo2f(xa[k]); xf[2 * k + 1] = hi2f(xa[k]); yf[2 * k] = lo2f(xb[k]); yf[2 * k + 1] = hi2f(xb[k]); }
    wave_ln_stats2(xf, yf, m0, d0, m1, d1);
    const float l0 = rbf(rbf(bf2f(xo0) - m0) / d0), l1 = rbf(rbf(bf2f(xo1) - m1) / d1);
    STAMP(9);
    float y[32];
#pragma unroll
    for (int k = 0; k < 4; ++k) {
      y[2 * k] = l0 * lo2f(wq0[k]); y[2 * k + 1] = l0 * hi2f(wq0[k]); y[8 + 2 * k] = l0 * lo2f(wq1[k]); y[8 + 2 * k + 1] = l0 * hi2f(wq1[k]);
      y[16 + 2 * k] = l1 * lo2f(wq0[k]); y[16 + 2 * k + 1] = l1 * hi2f(wq0[k]); y[24 + 2 * k] = l1 * lo2f(wq1[k]); y[24 + 2 * k + 1] = l1 * hi2f(wq1[k]);
    }
    STAMP(10);
#pragma unroll
    for (int i = 0; i < 16; ++i) y[i] = red32(y[i], y[i + 16]);
#pragma unroll
    for (int i = 0; i < 8; ++i) y[i] = red16(y[i], y[i + 8]);
#pragma unroll
    for (int i = 0; i < 4; ++i) y[i] = redd<ROR8>(y[i], y[i + 4], c & 8);
#pragma unroll
    for (int i = 0; i < 2; ++i) y[i] = redd<XOR7>(y[i], y[i + 2], c & 4);
    y[0] = redd<XOR2>(y[0], y[1], c & 2);
    y[0] += dpp<XOR1>(y[0]);
    red[wave * 32 + (lane >> 1)] = y[0];
  }
  STAMP(11);
  __syncthreads();
  if (tid < 16) {
    float s0 = 0.f, s1 = 0.f;
#pragma unroll
    for (int w = 0; w < NW; ++w) { f32x2 t = *reinterpret_cast<const f32x2*>(red + w * 32 + 2 * tid); s0 += t[0]; s1 += t[1]; }
    st_gran(ws + WS_QG + ((size_t)(tid >> 3) * NH + head) * 64 + 8 * g8 + (tid & 7), tag, f2bf(s0) | (f2bf(s1) << 16));
  }
  STAMP(12);
  SB();
  ISSUE(Bf, 1);
  SB();
  u64* const part_p = ws + WS_PART + ((size_t)b * 128 + head * 4 + split) * 512 + tid;
  u64* const ml_p = ws + WS_ML + (size_t)bh * 64 + (split * NW + wave) * 2;
  st_gran(part_p, epoch, 0u);
  if (lane < 2) st_gran(ml_p + lane, epoch, 0u);
  if (role == 1 && tid < 8) st_gran(ws + WS_HG + rcol0 + tid, epoch, 0u);
  if (role == 2 && tid < 16) st_gran(ws + WS_AG + rcol0 + tid, epoch, 0u);
  u32 qp[4];
  {
    const u64* q0 = ws + WS_QG + (size_t)bh * 64 + 4 * c;
    const u64* pp[4] = {q0, q0 + 1, q0 + 2, q0 + 3};
    poll_gran<4>(pp, tag, qp);
  }

  STAMP(1);
  float mx = -INFINITY;
#define COMPUTE_K(BUF, j_) do { float x_[8]; \
    _Pragma("unroll") for (int t_ = 0; t_ < 8; ++t_) { float d_ = dot2(BUF[t_][0], qp[0], 0.f); d_ = dot2(BUF[t_][1], qp[1], d_); \
      d_ = dot2(BUF[t_][2], qp[2], d_); x_[t_] = dot2(BUF[t_][3], qp[3], d_); } \
    _Pragma("unroll") for (int h_ = 0; h_ < 4; ++h_) x_[h_] = redd<ROR8>(x_[h_], x_[h_ + 4], c & 8); \
    _Pragma("unroll") for (int h_ = 0; h_ < 2; ++h_) x_[h_] = redd<XOR7>(x_[h_], x_[h_ + 2], c & 4); \
    x_[0] = redd<XOR2>(x_[0], x_[1], c & 2); \
    x_[0] += dpp<XOR1>(x_[0]); \
    float s_ = rbf(rbf(x_[0]) * SCALE) * LOG2E; \
    mx = fmaxf(mx, s_); \
    sc[(j_) * 32 + r * 8 + (c >> 1)] = s_; } while (0)

  SB();
  COMPUTE_K(A, 0); SB(); ISSUE(A, 2); SB();
  COMPUTE_K(Bf, 1); SB(); ISSUE(Bf, 3); SB();
  u32x4 RW[4];
  {
    const unsigned wdst = __builtin_amdgcn_readfirstlane((unsigned)(size_t)((LAS float*)lds + L_WO) + (unsigned)wave * 16384u);
    const u16* p = wo + (size_t)(head * DH + 16 * wave) * DM + 8 * lane;
#pragma unroll
    for (int i = 0; i < 16; ++i) glds16(p + (size_t)i * DM, wdst + i * 1024);
  }
  {
    const u16* p; size_t st;
    if (role == 0) {
      p = wd + (size_t)tid * DM + rcol0; st = (size_t)512 * DM;
    } else if (role == 2) {
      const int tt = tid & 255;
      p = (wave < 4 ? wg : wu) + (size_t)(tt >> 1) * FF + rcol0 + 8 * (tt & 1); st = (size_t)128 * FF;
    } else {
      p = wd; st = 0;
    }
#pragma unroll
    for (int i = 0; i < 3; ++i) RW[i] = *reinterpret_cast<const u32x4*>(p + (size_t)i * st);
    RW[3] = *reinterpret_cast<const u32x4*>(p + ((role == 0 && tid >= 256) ? (size_t)0 : 3 * st));
  }
  SB();
#pragma unroll 1
  for (int j = 2; j < NT; j += 2) {
    COMPUTE_K(A, j); SB(); ISSUE(A, j + 2); SB();
    COMPUTE_K(Bf, j + 1); SB(); ISSUE(Bf, j + 3); SB();
    __builtin_amdgcn_s_barrier(); SB();
  }
  STAMP(2);
  const float mw = wave_max(mx);
  {
    f32x4 s0 = *reinterpret_cast<const f32x4*>(sc + 8 * lane), s1 = *reinterpret_cast<const f32x4*>(sc + 8 * lane + 4);
    float l = 0.f;
#pragma unroll
    for (int k = 0; k < 4; ++k) { s0[k] = __builtin_amdgcn_exp2f(s0[k] - mw); s1[k] = __builtin_amdgcn_exp2f(s1[k] - mw); l += s0[k] + s1[k]; }
    *reinterpret_cast<f32x4*>(sc + 8 * lane) = s0;
    *reinterpret_cast<f32x4*>(sc + 8 * lane + 4) = s1;
    const float lw = wave_sum(l);
    if (lane < 2) st_gran(ml_p + lane, tag, __float_as_uint(lane == 0 ? mw : lw));
  }
  STAMP(3);

#define COMPUTE_V(BUF, j_) do { \
    f32x4 p0_ = *reinterpret_cast<const f32x4*>(sc + (j_) * 32 + r * 8), p1_ = *reinterpret_cast<const f32x4*>(sc + (j_) * 32 + r * 8 + 4); \
    _Pragma("unroll") for (int t_ = 0; t_ < 8; ++t_) { float p_ = t_ < 4 ? p0_[t_ & 3] : p1_[t_ & 3]; \
      asm volatile("" : "+v"(BUF[t_]));    \
      const f32x2 pp_ = {p_, p_}; \
      _Pragma("unroll") for (int k_ = 0; k_ < 4; ++k_) { \
        const f32x2 v_ = {lo2f(BUF[t_][k_]), hi2f(BUF[t_][k_])}; \
        acc2[k_] = __builtin_elementwise_fma(pp_, v_, acc2[k_]); } \
      if (t_ & 1) SB(); } } while (0)
  f32x2 acc2[4];
#pragma unroll
  for (int k = 0; k < 4; ++k) acc2[k] = f32x2{0.f, 0.f};
#pragma unroll 1
  for (int j = 0; j < NT - 2; j += 2) {
    COMPUTE_V(A, j); SB(); ISSUE(A, NT + j + 2); SB();
    COMPUTE_V(Bf, j + 1); SB(); ISSUE(Bf, NT + j + 3); SB();
    __builtin_amdgcn_s_barrier(); SB();
  }
  float fnorm;
  {
    const u64* pp[1] = {ws + WS_ML + (size_t)bh * 64 + lane};
    u32 v[1];
    poll_gran<1>(pp, tag, v);
    float val = __uint_as_float(v[0]);
    float other = dpp<XOR1>(val);
    float mj = (lane & 1) ? other : val, lj = (lane & 1) ? val : other;
    float M = wave_max(mj);
    float L = wave_sum((lane & 1) ? 0.f : __builtin_amdgcn_exp2f(mj - M) * lj);
    fnorm = __builtin_amdgcn_exp2f(mw - M) / L;
  }
  SB();
  COMPUTE_V(A, NT - 2);
  COMPUTE_V(Bf, NT - 1);
  STAMP(4);
  {
    float t0 = red16(acc2[0][0], acc2[1][0]), t1 = red16(acc2[0][1], acc2[1][1]);
    float t2 = red16(acc2[2][0], acc2[3][0]), t3 = red16(acc2[2][1], acc2[3][1]);
    float u0 = red32(t0, t2), u1 = red32(t1, t3);
    *reinterpret_cast<f32x2*>(lds + L_QP + wave * 128 + 8 * c + 2 * r) = f32x2{u0 * fnorm, u1 * fnorm};
  }
  __syncthreads();
  STAMP(5);
  {
    float o = lds[L_QP + r * 128 + 16 * wave + c] + lds[L_QP + (r + 4) * 128 + 16 * wave + c];
    o = xrow_sum(o);
    f32x2 a[4];
#pragma unroll
    for (int k = 0; k < 4; ++k) a[k] = f32x2{0.f, 0.f};
#pragma unroll
    for (int i = 0; i < 16; ++i) {
      fma8(a, __builtin_bit_cast(float, __builtin_amdgcn_readlane(__builtin_bit_cast(int, o), i)), wol[i * 64]);
      if ((i & 7) == 7) FENCE();
    }
    *reinterpret_cast<f32x4*>(sc + 8 * lane) = f32x4{a[0][0], a[0][1], a[1][0], a[1][1]};
    *reinterpret_cast<f32x4*>(sc + 8 * lane + 4) = f32x4{a[2][0], a[2][1], a[3][0], a[3][1]};
  }
  __syncthreads();
  {
    float s = 0.f;
#pragma unroll
    for (int w = 0; w < NW; ++w) s += lds[L_SC + w * 512 + tid];
    st_gran(part_p, tag, __float_as_uint(s));
  }
  STAMP(6);

  if (role == 1) {
    const int seg = tid >> 1, half = tid & 1;
    const u64* p0 = ws + WS_PART + (size_t)seg * 512 + rcol0 + 4 * half;
    const u64* pp[4] = {p0, p0 + 1, p0 + 2, p0 + 3};
    const float xr0 = bf2f(x[rcol0 + (tid & 7)]), xr1 = bf2f(x[DM + rcol0 + (tid & 7)]);
    u32 v[4];
    poll_gran<4>(pp, tag, v);
    STAMP(7);
    float f0 = red32(__uint_as_float(v[0]), __uint_as_float(v[2])), f1 = red32(__uint_as_float(v[1]), __uint_as_float(v[3]));
    f0 = red16(f0, f1);
    f0 += dpp<ROR8>(f0); f0 += dpp<ROR4>(f0); f0 += dpp<XOR2>(f0);
    if ((lane & 14) == 0) red[wave * 8 + 4 * half + (lane >> 4)] = f0;
    __syncthreads();
    if (tid < 8) {
      float s0 = (red[tid] + red[8 + tid]) + (red[16 + tid] + red[24 + tid]);
      float s1 = (red[32 + tid] + red[40 + tid]) + (red[48 + tid] + red[56 + tid]);
      u32 h0 = f2bf(xr0 + rbf(s0));
      u32 h1 = f2bf(xr1 + rbf(s1));
      st_gran(ws + WS_HG + rcol0 + tid, tag, h0 | (h1 << 16));
    }
    STAMP(8);
  } else if (role == 2) {
    float* const p_h = lds + L_WO;
    {
      const u64* pp[1] = {ws + WS_HG + tid};
      u32 v[1];
      poll_gran<1>(pp, tag, v);
      STAMP(7);
      p_h[tid] = lo2f(v[0]);
      p_h[512 + tid] = hi2f(v[0]);
    }
    __syncthreads();
    {
      const int tt = tid & 255;
      float hx[8], hy[8], m0, d0, m1, d1;
      {
        f32x4 a0 = *reinterpret_cast<const f32x4*>(p_h + 8 * lane), a1 = *reinterpret_cast<const f32x4*>(p_h + 8 * lane + 4);
        f32x4 b0 = *reinterpret_cast<const f32x4*>(p_h + 512 + 8 * lane), b1 = *reinterpret_cast<const f32x4*>(p_h + 512 + 8 * lane + 4);
#pragma unroll
        for (int k = 0; k < 4; ++k) { hx[k] = a0[k]; hx[4 + k] = a1[k]; hy[k] = b0[k]; hy[4 + k] = b1[k]; }
      }
      float h0[4], h1[4];
#pragma unroll
      for (int i = 0; i < 4; ++i) { h0[i] = p_h[(tt >> 1) + 128 * i]; h1[i] = p_h[512 + (tt >> 1) + 128 * i]; }
      wave_ln_stats2(hx, hy, m0, d0, m1, d1);
      f32x2 a0[4], a1[4];
#pragma unroll
      for (int k = 0; k < 4; ++k) { a0[k] = f32x2{0.f, 0.f}; a1[k] = f32x2{0.f, 0.f}; }
#pragma unroll
      for (int i = 0; i < 4; ++i) {
        fma8(a0, rbf(rbf(h0[i] - m0) / d0), RW[i]);
        fma8(a1, rbf(rbf(h1[i] - m1) / d1), RW[i]);
      }
      float y[8];
#pragma unroll
      for (int k = 0; k < 4; ++k) { y[2 * k] = red32(a0[k][0], a1[k][0]); y[2 * k + 1] = red32(a0[k][1], a1[k][1]); }
#pragma unroll
      for (int k = 0; k < 4; ++k) y[k] = red16(y[k], y[k + 4]);
#pragma unroll
      for (int k = 0; k < 2; ++k) y[k] = redd<ROR8>(y[k], y[k + 2], c & 8);
      {
        const float s0 = y[0] + dpp<XOR1>(dpp<XOR2>(dpp<XOR7>(y[0]))), s1 = y[1] + dpp<XOR1>(dpp<XOR2>(dpp<XOR7>(y[1])));
        y[0] = (c & 4) ? s1 : s0;
      }
      y[0] += dpp<XOR2>(y[0]);
      if ((lane & 2) == 0) red[wave * 32 + (lane >> 5) * 16 + (lane & 1) * 8 + ((lane >> 2) & 7)] = y[0];
    }
    __syncthreads();
    if (tid < 16) {
      u32 pk = 0;
#pragma unroll
      for (int bb = 0; bb < 2; ++bb) {
        float g = (red[0 * 32 + bb * 16 + tid] + red[1 * 32 + bb * 16 + tid]) + (red[2 * 32 + bb * 16 + tid] + red[3 * 32 + bb * 16 + tid]);
        float u = (red[4 * 32 + bb * 16 + tid] + red[5 * 32 + bb * 16 + tid]) + (red[6 * 32 + bb * 16 + tid] + red[7 * 32 + bb * 16 + tid]);
        g = rbf(g); u = rbf(u);
        float sg = rbf(g / (1.f + __expf(-g)));
        pk |= f2bf(sg * u) << (16 * bb);
      }
      st_gran(ws + WS_AG + rcol0 + tid, tag, pk);
    }
    STAMP(8);
  } else if (role == 0) {
    const u64* ap = ws + WS_AG + tid;
    const u64* pp[5] = {ap, ap + 512, ap + 1024, tid < 256 ? ap + 1536 : ap, ws + WS_HG + rcol0 + (tid & 7)};
    u32 v[5];
    poll_gran<5>(pp, tag, v);
    STAMP(7);
    f32x2 a0[4], a1[4];
#pragma unroll
    for (int k = 0; k < 4; ++k) { a0[k] = f32x2{0.f, 0.f}; a1[k] = f32x2{0.f, 0.f}; }
#pragma unroll
    for (int i = 0; i < 4; ++i) {
      float l0 = lo2f(v[i]), l1 = hi2f(v[i]);
      if (i == 3 && tid >= 256) { l0 = 0.f; l1 = 0.f; }
      fma8(a0, l0, RW[i]);
      fma8(a1, l1, RW[i]);
    }
    float y[8];
#pragma unroll
    for (int k = 0; k < 4; ++k) { y[2 * k] = red32(a0[k][0], a1[k][0]); y[2 * k + 1] = red32(a0[k][1], a1[k][1]); }
#pragma unroll
    for (int k = 0; k < 4; ++k) y[k] = red16(y[k], y[k + 4]);
#pragma unroll
    for (int k = 0; k < 2; ++k) y[k] = redd<ROR8>(y[k], y[k + 2], c & 8);
    y[0] = redd<XOR7>(y[0], y[1], c & 4);
    y[0] += dpp<XOR2>(y[0]);
    y[0] += dpp<XOR1>(y[0]);
    if ((lane & 3) == 0) red[wave * 16 + (lane >> 2)] = y[0];
    __syncthreads();
    if (tid < 16) {
      float s = 0.f;
#pragma unroll
      for (int w = 0; w < NW; ++w) s += red[w * 16 + tid];
      float hh = (tid < 8) ? lo2f(v[4]) : hi2f(v[4]);
      out[(tid >> 3) * DM + rcol0 + (tid & 7)] = (u16)f2bf(hh + rbf(s));
    }
    STAMP(8);
    if (bid == 0 && tid == 0) __hip_atomic_store((u32*)(ws + WS_E), tag, __ATOMIC_RELAXED, __HIP_MEMORY_SCOPE_AGENT);
  }
}

extern "C" void kernel_launch(void* const* d_in, const int* in_sizes, int n_in,
                              void* d_out, int out_size, void* d_ws, size_t ws_size,
                              hipStream_t stream) {
  fused_kernel<<<256, NTHR, 0, stream>>>((const u16*)d_in[0], (const u16*)d_in[1], (const u16*)d_in[2], (const u16*)d_in[3],
                                         (const u16*)d_in[4], (const u16*)d_in[6], (const u16*)d_in[7], (const u16*)d_in[8],
                                         (u16*)d_out, (u64*)d_ws);
}
```

```cpp
#include <hip/hip_runtime.h>
#include <hip/hip_bf16.h>
#include <stdint.h>

typedef unsigned int u32;
typedef unsigned short u16;
typedef unsigned long long u64;
typedef __attribute__((ext_vector_type(4))) unsigned u32x4;
typedef __attribute__((ext_vector_type(4))) float f32x4;
typedef __attribute__((ext_vector_type(2))) float f32x2;
typedef __attribute__((ext_vector_type(2))) __bf16 bf16x2;
typedef __attribute__((ext_vector_type(4))) __bf16 bf16x4;
typedef __attribute__((ext_vector_type(8))) __bf16 bf16x8;

constexpr int NB = 2, DM = 512, NH = 32, SK = 16384, DH = 128, FF = 1792;
constexpr int NSPLIT = 4;
constexpr int NW = 8;
constexpr int NTHR = NW * 64;
constexpr int KPW = SK / NSPLIT / NW;
constexpr int TK = 32;
constexpr int NT = KPW / TK;
constexpr float SCALE = 0.08838834764831845f;
constexpr float LOG2E = 1.4426950408889634f;
constexpr unsigned SPIN_CAP = 1u << 22;

constexpr size_t WS_E = 0;
constexpr size_t WS_QG = 64;
constexpr size_t WS_ML = WS_QG + 64 * 64;
constexpr size_t WS_PART = WS_ML + 64 * 64;
constexpr size_t WS_HG = WS_PART + 2 * 128 * 512;
constexpr size_t WS_AG = WS_HG + 8 * 512;

__device__ __forceinline__ float bf2f(u32 v) { return __uint_as_float(v << 16); }
__device__ __forceinline__ float lo2f(u32 v) { return __uint_as_float(v << 16); }
__device__ __forceinline__ float hi2f(u32 v) { return __uint_as_float(v & 0xffff0000u); }
__device__ __forceinline__ float rbf(float x) { return (float)(__bf16)x; }
__device__ __forceinline__ u32 f2bf(float x) { __bf16 b = (__bf16)x; return (u32)__builtin_bit_cast(u16, b); }
__device__ __forceinline__ float dot2(u32 a, u32 b, float acc) {
  return __builtin_amdgcn_fdot2_f32_bf16(__builtin_bit_cast(bf16x2, a), __builtin_bit_cast(bf16x2, b), acc, false);
}
template <int CTRL> __device__ __forceinline__ float dpp(float x) {
  return __builtin_bit_cast(float, __builtin_amdgcn_mov_dpp(__builtin_bit_cast(int, x), CTRL, 0xf, 0xf, true));
}
constexpr int XOR1 = 0xB1, XOR2 = 0x4E, XOR7 = 0x141, ROR8 = 0x128, ROR4 = 0x124, ROR2 = 0x122, ROR1 = 0x121;
__device__ __forceinline__ float red32(float a, float b) {
  auto t = __builtin_amdgcn_permlane32_swap(__float_as_uint(a), __float_as_uint(b), false, false);
  return __uint_as_float(t[0]) + __uint_as_float(t[1]);
}
__device__ __forceinline__ float red16(float a, float b) {
  auto t = __builtin_amdgcn_permlane16_swap(__float_as_uint(a), __float_as_uint(b), false, false);
  return __uint_as_float(t[0]) + __uint_as_float(t[1]);
}
template <int CTRL> __device__ __forceinline__ float redd(float a, float b, bool hi) {
  const float t1 = a + dpp<CTRL>(a), t2 = b + dpp<CTRL>(b);
  return hi ? t2 : t1;
}
__device__ __forceinline__ float xrow_sum(float x) { x = red16(x, x); return red32(x, x); }
__device__ __forceinline__ float xrow_max(float x) {
  auto s = __builtin_amdgcn_permlane16_swap(__float_as_uint(x), __float_as_uint(x), false, false);
  x = fmaxf(__uint_as_float(s[0]), __uint_as_float(s[1]));
  auto t = __builtin_amdgcn_permlane32_swap(__float_as_uint(x), __float_as_uint(x), false, false);
  return fmaxf(__uint_as_float(t[0]), __uint_as_float(t[1]));
}
__device__ __forceinline__ float wave_sum(float x) {
  x += dpp<ROR8>(x); x += dpp<ROR4>(x); x += dpp<ROR2>(x); x += dpp<ROR1>(x);
  return xrow_sum(x);
}
__device__ __forceinline__ float wave_max(float x) {
  x = fmaxf(x, dpp<ROR8>(x)); x = fmaxf(x, dpp<ROR4>(x)); x = fmaxf(x, dpp<ROR2>(x)); x = fmaxf(x, dpp<ROR1>(x));
  return xrow_max(x);
}
__device__ __forceinline__ void st_gran(u64* p, u32 tag, u32 val) {
  __hip_atomic_store(p, ((u64)tag << 32) | (u64)val, __ATOMIC_RELAXED, __HIP_MEMORY_SCOPE_AGENT);
}
__device__ __forceinline__ u64 ld_gran(const u64* p) {
  return __hip_atomic_load(p, __ATOMIC_RELAXED, __HIP_MEMORY_SCOPE_AGENT);
}
template <int N> __device__ __forceinline__ void poll_gran(const u64* const (&p)[N], u32 tag, u32 (&v)[N]) {
  for (unsigned spins = 0;; ++spins) {
    bool ok = true;
#pragma unroll
    for (int k = 0; k < N; ++k) { u64 g = ld_gran(p[k]); v[k] = (u32)g; ok &= ((u32)(g >> 32) == tag); }
    if (__all(ok) || spins > SPIN_CAP) break;
    __builtin_amdgcn_s_sleep(1);
  }
}
__device__ __forceinline__ float fdiv(float a, float d, float r) { const float q = a * r; return fmaf(fmaf(-q, d, a), r, q); }
__device__ __forceinline__ void fma8(f32x2 (&a)[4], float s, u32x4 w) {
  const f32x2 ss = {s, s};
#pragma unroll
  for (int k = 0; k < 4; ++k) { const f32x2 v = {lo2f(w[k]), hi2f(w[k])}; a[k] = __builtin_elementwise_fma(ss, v, a[k]); }
  asm volatile("" : "+v"(a[0]), "+v"(a[1]), "+v"(a[2]), "+v"(a[3]));
}
__device__ __forceinline__ void wave_ln_stats2(const float (&x)[8], const float (&y)[8], float& m0, float& d0, float& m1, float& d1) {
  float s0 = ((x[0] + x[1]) + (x[2] + x[3])) + ((x[4] + x[5]) + (x[6] + x[7]));
  float s1 = ((y[0] + y[1]) + (y[2] + y[3])) + ((y[4] + y[5]) + (y[6] + y[7]));
  s0 = wave_sum(s0) * (1.f / 512.f); s1 = wave_sum(s1) * (1.f / 512.f);
  float v0 = 0.f, v1 = 0.f;
#pragma unroll
  for (int k = 0; k < 8; ++k) { v0 += (x[k] - s0) * (x[k] - s0); v1 += (y[k] - s1) * (y[k] - s1); }
  v0 = wave_sum(v0) * (1.f / 512.f); v1 = wave_sum(v1) * (1.f / 512.f);
  m0 = rbf(s0); m1 = rbf(s1);
  d0 = rbf(sqrtf(rbf(rbf(v0) + 1e-5f))); d1 = rbf(sqrtf(rbf(rbf(v1) + 1e-5f)));
}

#define SB() __builtin_amdgcn_sched_barrier(0)
#define LAS __attribute__((address_space(3)))
#define FENCE() do { asm volatile("" ::: "memory"); __builtin_amdgcn_sched_barrier(0); } while (0)
__device__ __forceinline__ void glds16(const void* gsrc, unsigned lds_dst) {
  unsigned keep;
  asm volatile("s_mov_b32 %0, m0\n\ts_mov_b32 m0, %2\n\ts_nop 0\n\tglobal_load_lds_dwordx4 %1, off\n\ts_mov_b32 m0, %0" : "=&s"(keep) : "v"(gsrc), "s"(lds_dst) : "memory");
}
#define STAMP(i_) do {} while (0)
constexpr int L_SC = 0;
constexpr int L_QP = L_SC + NW * 512;
constexpr int L_RED = L_QP + NW * 128;
constexpr int L_OI = L_RED + 256;
constexpr int L_WO = L_OI + 192;
constexpr int L_TOT = L_WO + NW * 16 * 256;

__global__ void __launch_bounds__(NTHR, 2) fused_kernel(const u16* __restrict__ x, const u16* __restrict__ kc, const u16* __restrict__ vc,
    const u16* __restrict__ wq, const u16* __restrict__ wo, const u16* __restrict__ wg, const u16* __restrict__ wu,
    const u16* __restrict__ wd, u16* __restrict__ out, u64* __restrict__ ws) {
  __shared__ __attribute__((aligned(16))) float lds[L_TOT];
  const int tid = threadIdx.x, lane = tid & 63, r = lane >> 4, c = lane & 15;
  const int wave = __builtin_amdgcn_readfirstlane(tid >> 6);
  const int bid = blockIdx.x, xcd = bid & 7, idx = bid >> 3;
  const int head = xcd * 4 + (idx & 3), g8 = idx >> 2, b = g8 & 1, split = g8 >> 1;
  const int bh = b * NH + head;
  const int role = idx < 8 ? 0 : (idx < 16 ? 1 : (idx < 30 ? 2 : 3));
  const int rcol0 = role == 0 ? 8 * (xcd * 8 + idx) : (role == 2 ? 16 * (xcd * 14 + idx - 16) : 8 * (xcd * 8 + ((idx - 8) & 7)));
  float* const sc = lds + L_SC + wave * 512;
  float* const red = lds + L_RED;

  STAMP(0);
  const u32 epoch = (u32)__hip_atomic_load((const u32*)(ws + WS_E), __ATOMIC_RELAXED, __HIP_MEMORY_SCOPE_AGENT);
  const u32x4 xa = *reinterpret_cast<const u32x4*>(x + 8 * lane), xb = *reinterpret_cast<const u32x4*>(x + DM + 8 * lane);
  const u32 xo0 = x[tid], xo1 = x[DM + tid];
  const u32x4 wq0 = *reinterpret_cast<const u32x4*>(wq + (size_t)tid * 4096 + head * DH + 16 * g8);
  const u32x4 wq1 = *reinterpret_cast<const u32x4*>(wq + (size_t)tid * 4096 + head * DH + 16 * g8 + 8);
  const size_t base = ((size_t)bh * SK + (size_t)split * (SK / NSPLIT) + (size_t)wave * TK) * DH + (size_t)lane * 8;
  const u16* kb = kc + base;
  const u16* vb = vc + base;
  u32x4 A[8], Bf[8];
#define ISSUE(BUF, j_) do { const u16* s_ = ((j_) < NT ? kb : vb) + (size_t)((j_) & (NT - 1)) * (NW * TK * DH); \
    _Pragma("unroll") for (int t_ = 0; t_ < 8; ++t_) BUF[t_] = __builtin_nontemporal_load(reinterpret_cast<const u32x4*>(s_ + t_ * 4 * DH)); } while (0)
  SB();
  __builtin_amdgcn_s_barrier();
  SB();
  ISSUE(A, 0);
  SB();

  const u32 tag = epoch + 1u;
  {
    float xf[8], yf[8], m0, d0, m1, d1;
#pragma unroll
    for (int k = 0; k < 4; ++k) { xf[2 * k] = lo2f(xa[k]); xf[2 * k + 1] = hi2f(xa[k]); yf[2 * k] = lo2f(xb[k]); yf[2 * k + 1] = hi2f(xb[k]); }
    wave_ln_stats2(xf, yf, m0, d0, m1, d1);
    const float l0 = rbf(rbf(bf2f(xo0) - m0) / d0), l1 = rbf(rbf(bf2f(xo1) - m1) / d1);
    STAMP(9);
    float y[32];
#pragma unroll
    for (int k = 0; k < 4; ++k) {
      y[2 * k] = l0 * lo2f(wq0[k]); y[2 * k + 1] = l0 * hi2f(wq0[k]); y[8 + 2 * k] = l0 * lo2f(wq1[k]); y[8 + 2 * k + 1] = l0 * hi2f(wq1[k]);
      y[16 + 2 * k] = l1 * lo2f(wq0[k]); y[16 + 2 * k + 1] = l1 * hi2f(wq0[k]); y[24 + 2 * k] = l1 * lo2f(wq1[k]); y[24 + 2 * k + 1] = l1 * hi2f(wq1[k]);
    }
    STAMP(10);
#pragma unroll
    for (int i = 0; i < 16; ++i) y[i] = red32(y[i], y[i + 16]);
#pragma unroll
    for (int i = 0; i < 8; ++i) y[i] = red16(y[i], y[i + 8]);
#pragma unroll
    for (int i = 0; i < 4; ++i) y[i] = redd<ROR8>(y[i], y[i + 4], c & 8);
#pragma unroll
    for (int i = 0; i < 2; ++i) y[i] = redd<XOR7>(y[i], y[i + 2], c & 4);
    y[0] = redd<XOR2>(y[0], y[1], c & 2);
    y[0] += dpp<XOR1>(y[0]);
    red[wave * 32 + (lane >> 1)] = y[0];
  }
  STAMP(11);
  __syncthreads();
  if (tid < 16) {
    float s0 = 0.f, s1 = 0.f;
#pragma unroll
    for (int w = 0; w < NW; ++w) { f32x2 t = *reinterpret_cast<const f32x2*>(red + w * 32 + 2 * tid); s0 += t[0]; s1 += t[1]; }
    st_gran(ws + WS_QG + ((size_t)(tid >> 3) * NH + head) * 64 + 8 * g8 + (tid & 7), tag, f2bf(s0) | (f2bf(s1) << 16));
  }
  STAMP(12);
  SB();
  ISSUE(Bf, 1);
  SB();
  u64* const part_p = ws + WS_PART + ((size_t)b * 128 + head * 4 + split) * 512 + wave * 64 + c;
  u64* const ml_p = ws + WS_ML + (size_t)bh * 64 + (split * NW + wave) * 2;
  if (lane < 16) {
#pragma unroll
    for (int nt = 0; nt < 4; ++nt) st_gran(part_p + 16 * nt, epoch, 0u);
  }
  if (tid < 64) lds[L_OI + 128 + tid] = 0.f;
  if (lane < 2) st_gran(ml_p + lane, epoch, 0u);
  if (role == 1 && tid < 64) st_gran(ws + WS_HG + (tid >> 3) * 512 + rcol0 + (tid & 7), epoch, 0u);
  if (role == 2 && tid < 64) { st_gran(ws + WS_AG + (tid >> 4) * FF + rcol0 + c, epoch, 0u); st_gran(ws + WS_AG + ((tid >> 4) + 4) * FF + rcol0 + c, epoch, 0u); }
  u32 qp[4];
  {
    const u64* q0 = ws + WS_QG + (size_t)bh * 64 + 4 * c;
    const u64* pp[4] = {q0, q0 + 1, q0 + 2, q0 + 3};
    poll_gran<4>(pp, tag, qp);
  }

  STAMP(1);
  float mx = -INFINITY;
#define COMPUTE_K(BUF, j_) do { float x_[8]; \
    _Pragma("unroll") for (int t_ = 0; t_ < 8; ++t_) { float d_ = dot2(BUF[t_][0], qp[0], 0.f); d_ = dot2(BUF[t_][1], qp[1], d_); \
      d_ = dot2(BUF[t_][2], qp[2], d_); x_[t_] = dot2(BUF[t_][3], qp[3], d_); } \
    _Pragma("unroll") for (int h_ = 0; h_ < 4; ++h_) x_[h_] = redd<ROR8>(x_[h_], x_[h_ + 4], c & 8); \
    _Pragma("unroll") for (int h_ = 0; h_ < 2; ++h_) x_[h_] = redd<XOR7>(x_[h_], x_[h_ + 2], c & 4); \
    x_[0] = redd<XOR2>(x_[0], x_[1], c & 2); \
    x_[0] += dpp<XOR1>(x_[0]); \
    float s_ = rbf(rbf(x_[0]) * SCALE) * LOG2E; \
    mx = fmaxf(mx, s_); \
    sc[(j_) * 32 + r * 8 + (c >> 1)] = s_; } while (0)

  SB();
  COMPUTE_K(A, 0); SB(); ISSUE(A, 2); SB();
  COMPUTE_K(Bf, 1); SB(); ISSUE(Bf, 3); SB();
  u32x4 RW[4];
  {
    const unsigned wdst = __builtin_amdgcn_readfirstlane((unsigned)(size_t)((LAS float*)lds + L_WO) + (unsigned)wave * 16384u);
    const u16* p = wo + (size_t)(head * DH + 16 * wave) * DM;
#pragma unroll
    for (int i = 0; i < 16; ++i) {
      const int sw = 2 * ((i & 3) | (((i >> 3) & 1) << 2));
      glds16(p + (size_t)i * DM + 8 * (lane ^ sw), wdst + i * 1024);
    }
  }
  {
    const u16* p; size_t st;
    if (role == 0) {
      p = wd + (size_t)tid * DM + rcol0; st = (size_t)512 * DM;
    } else {
      p = wd; st = 0;
    }
#pragma unroll
    for (int i = 0; i < 3; ++i) RW[i] = *reinterpret_cast<const u32x4*>(p + (size_t)i * st);
    RW[3] = *reinterpret_cast<const u32x4*>(p + ((role == 0 && tid >= 256) ? (size_t)0 : 3 * st));
  }
  u16 GW[2][2][8];
  {
    const size_t goff = role == 2 ? (size_t)(64 * wave + 8 * r) * FF + rcol0 + c : (size_t)0;
    const size_t gst = role == 2 ? (size_t)FF : (size_t)0;
#pragma unroll
    for (int ks = 0; ks < 2; ++ks)
#pragma unroll
      for (int j = 0; j < 8; ++j) { GW[0][ks][j] = wg[goff + (32 * ks + j) * gst]; GW[1][ks][j] = wu[goff + (32 * ks + j) * gst]; }
  }
  SB();
#pragma unroll 1
  for (int j = 2; j < NT; j += 2) {
    COMPUTE_K(A, j); SB(); ISSUE(A, j + 2); SB();
    __builtin_amdgcn_s_barrier(); SB();
    COMPUTE_K(Bf, j + 1); SB(); ISSUE(Bf, j + 3); SB();
    __builtin_amdgcn_s_barrier(); SB();
  }
  STAMP(2);
  const float mw = wave_max(mx);
  {
    f32x4 s0 = *reinterpret_cast<const f32x4*>(sc + 8 * lane), s1 = *reinterpret_cast<const f32x4*>(sc + 8 * lane + 4);
    float l = 0.f;
#pragma unroll
    for (int k = 0; k < 4; ++k) { s0[k] = __builtin_amdgcn_exp2f(s0[k] - mw); s1[k] = __builtin_amdgcn_exp2f(s1[k] - mw); l += s0[k] + s1[k]; }
    *reinterpret_cast<f32x4*>(sc + 8 * lane) = s0;
    *reinterpret_cast<f32x4*>(sc + 8 * lane + 4) = s1;
    const float lw = wave_sum(l);
    if (lane < 2) st_gran(ml_p + lane, tag, __float_as_uint(lane == 0 ? mw : lw));
  }
  STAMP(3);

#define COMPUTE_V(BUF, j_) do { \
    f32x4 p0_ = *reinterpret_cast<const f32x4*>(sc + (j_) * 32 + r * 8), p1_ = *reinterpret_cast<const f32x4*>(sc + (j_) * 32 + r * 8 + 4); \
    _Pragma("unroll") for (int t_ = 0; t_ < 8; ++t_) { float p_ = t_ < 4 ? p0_[t_ & 3] : p1_[t_ & 3]; \
      asm volatile("" : "+v"(BUF[t_]));    \
      const f32x2 pp_ = {p_, p_}; \
      _Pragma("unroll") for (int k_ = 0; k_ < 4; ++k_) { \
        const f32x2 v_ = {lo2f(BUF[t_][k_]), hi2f(BUF[t_][k_])}; \
        acc2[k_] = __builtin_elementwise_fma(pp_, v_, acc2[k_]); } \
      if (t_ & 1) SB(); } } while (0)
  f32x2 acc2[4];
#pragma unroll
  for (int k = 0; k < 4; ++k) acc2[k] = f32x2{0.f, 0.f};
#pragma unroll 1
  for (int j = 0; j < NT - 2; j += 2) {
    COMPUTE_V(A, j); SB(); ISSUE(A, NT + j + 2); SB();
    __builtin_amdgcn_s_barrier(); SB();
    COMPUTE_V(Bf, j + 1); SB(); ISSUE(Bf, NT + j + 3); SB();
    __builtin_amdgcn_s_barrier(); SB();
  }
  bf16x8 WB[4][4];
  {
    const LAS char* wbase = (const LAS char*)((LAS float*)lds + L_WO);
    const int q_ = c >> 2, p_ = c & 3;
#pragma unroll
    for (int ks = 0; ks < 4; ++ks)
#pragma unroll
      for (int nt = 0; nt < 4; ++nt) {
        const int sw = 2 * (q_ | ((r & 1) << 2));
        const int ch = (2 * (4 * wave + nt) + (p_ >> 1)) ^ sw;
        const int row = 32 * ks + 8 * r + q_;
        const bf16x4 lo = __builtin_amdgcn_ds_read_tr16_b64_v4bf16((LAS bf16x4*)(wbase + row * 1024 + ch * 16 + 8 * (p_ & 1)));
        const bf16x4 hi = __builtin_amdgcn_ds_read_tr16_b64_v4bf16((LAS bf16x4*)(wbase + (row + 4) * 1024 + ch * 16 + 8 * (p_ & 1)));
        WB[ks][nt] = __builtin_shufflevector(lo, hi, 0, 1, 2, 3, 4, 5, 6, 7);
      }
  }
  float fnorm;
  {
    const u64* pp[1] = {ws + WS_ML + (size_t)bh * 64 + lane};
    u32 v[1];
    poll_gran<1>(pp, tag, v);
    float val = __uint_as_float(v[0]);
    float other = dpp<XOR1>(val);
    float mj = (lane & 1) ? other : val, lj = (lane & 1) ? val : other;
    float M = wave_max(mj);
    float L = wave_sum((lane & 1) ? 0.f : __builtin_amdgcn_exp2f(mj - M) * lj);
    fnorm = __builtin_amdgcn_exp2f(mw - M) / L;
  }
  SB();
  COMPUTE_V(A, NT - 2);
  COMPUTE_V(Bf, NT - 1);
  STAMP(4);
  {
    float t0 = red16(acc2[0][0], acc2[1][0]), t1 = red16(acc2[0][1], acc2[1][1]);
    float t2 = red16(acc2[2][0], acc2[3][0]), t3 = red16(acc2[2][1], acc2[3][1]);
    float u0 = red32(t0, t2), u1 = red32(t1, t3);
    *reinterpret_cast<f32x2*>(lds + L_QP + wave * 128 + 8 * c + 2 * r) = f32x2{u0 * fnorm, u1 * fnorm};
  }
  __syncthreads();
  STAMP(5);
  if (tid < 128) {
    float o = 0.f;
#pragma unroll
    for (int w = 0; w < NW; ++w) o += lds[L_QP + w * 128 + tid];
    const u32 hi = f2bf(o), lo = f2bf(o - bf2f(hi));
    u16* oi = reinterpret_cast<u16*>(lds + L_OI);
    oi[tid] = (u16)hi; oi[128 + tid] = (u16)lo;
  }
  __syncthreads();
  {
    const u16* oi = reinterpret_cast<const u16*>(lds + L_OI) + (c < 2 ? c : 2) * 128 + 8 * r;
    f32x4 D[4];
#pragma unroll
    for (int nt = 0; nt < 4; ++nt) D[nt] = f32x4{0.f, 0.f, 0.f, 0.f};
#pragma unroll
    for (int ks = 0; ks < 4; ++ks) {
      const bf16x8 af = *reinterpret_cast<const bf16x8*>(oi + 32 * ks);
#pragma unroll
      for (int nt = 0; nt < 4; ++nt) D[nt] = __builtin_amdgcn_mfma_f32_16x16x32_bf16(af, WB[ks][nt], D[nt], 0, 0, 0);
    }
    if (lane < 16) {
#pragma unroll
      for (int nt = 0; nt < 4; ++nt) st_gran(part_p + 16 * nt, tag, __float_as_uint(D[nt][0] + D[nt][1]));
    }
  }
  STAMP(6);

  if (role == 1) {
    const int seg = tid >> 1, half = tid & 1;
    const u64* p0 = ws + WS_PART + (size_t)seg * 512 + rcol0 + 4 * half;
    const u64* pp[4] = {p0, p0 + 1, p0 + 2, p0 + 3};
    const float xr0 = bf2f(x[rcol0 + (tid & 7)]), xr1 = bf2f(x[DM + rcol0 + (tid & 7)]);
    u32 v[4];
    poll_gran<4>(pp, tag, v);
    STAMP(7);
    float f0 = red32(__uint_as_float(v[0]), __uint_as_float(v[2])), f1 = red32(__uint_as_float(v[1]), __uint_as_float(v[3]));
    f0 = red16(f0, f1);
    f0 += dpp<ROR8>(f0); f0 += dpp<ROR4>(f0); f0 += dpp<XOR2>(f0);
    if ((lane & 14) == 0) red[wave * 8 + 4 * half + (lane >> 4)] = f0;
    __syncthreads();
    if (tid < 64) {
      const int e = tid & 7;
      float s0 = (red[e] + red[8 + e]) + (red[16 + e] + red[24 + e]);
      float s1 = (red[32 + e] + red[40 + e]) + (red[48 + e] + red[56 + e]);
      u32 h0 = f2bf(xr0 + rbf(s0));
      u32 h1 = f2bf(xr1 + rbf(s1));
      st_gran(ws + WS_HG + (tid >> 3) * 512 + rcol0 + e, tag, h0 | (h1 << 16));
    }
    STAMP(8);
  } else if (role == 2) {
    float* const p_st = lds + L_QP;
    u16* const p_ln = reinterpret_cast<u16*>(lds + L_SC);
    bf16x8 GB[2][2];
#pragma unroll
    for (int mt = 0; mt < 2; ++mt)
#pragma unroll
      for (int ks = 0; ks < 2; ++ks) {
        u32x4 t;
#pragma unroll
        for (int j = 0; j < 4; ++j) t[j] = (u32)GW[mt][ks][2 * j] | ((u32)GW[mt][ks][2 * j + 1] << 16);
        GB[mt][ks] = __builtin_bit_cast(bf16x8, t);
      }
    p_ln[1024 + tid] = 0;
    float h0, h1;
    {
      const u64* pp[1] = {ws + WS_HG + xcd * 512 + tid};
      u32 v[1];
      poll_gran<1>(pp, tag, v);
      STAMP(7);
      h0 = lo2f(v[0]); h1 = hi2f(v[0]);
      const float mu0 = wave_sum(h0) * (1.f / 64.f), mu1 = wave_sum(h1) * (1.f / 64.f);
      const float q0 = wave_sum((h0 - mu0) * (h0 - mu0)), q1 = wave_sum((h1 - mu1) * (h1 - mu1));
      if (lane == 0) *reinterpret_cast<f32x4*>(p_st + 4 * wave) = f32x4{mu0, q0, mu1, q1};
    }
    __syncthreads();
    {
      f32x4 st[8];
#pragma unroll
      for (int w = 0; w < NW; ++w) st[w] = *reinterpret_cast<const f32x4*>(p_st + 4 * w);
      float m0 = 0.f, m1 = 0.f, v0 = 0.f, v1 = 0.f;
#pragma unroll
      for (int w = 0; w < NW; ++w) { m0 += st[w][0]; m1 += st[w][2]; v0 += st[w][1]; v1 += st[w][3]; }
      m0 *= 0.125f; m1 *= 0.125f;
#pragma unroll
      for (int w = 0; w < NW; ++w) { v0 = fmaf(64.f * (st[w][0] - m0), st[w][0] - m0, v0); v1 = fmaf(64.f * (st[w][2] - m1), st[w][2] - m1, v1); }
      const float d0 = rbf(sqrtf(rbf(rbf(v0 * (1.f / 512.f)) + 1e-5f))), d1 = rbf(sqrtf(rbf(rbf(v1 * (1.f / 512.f)) + 1e-5f)));
      p_ln[tid] = (u16)f2bf(rbf(h0 - rbf(m0)) / d0);
      p_ln[512 + tid] = (u16)f2bf(rbf(h1 - rbf(m1)) / d1);
    }
    __syncthreads();
    {
      const u16* ap = p_ln + (c < 2 ? c : 2) * 512 + 64 * wave + 8 * r;
      const bf16x8 a0 = *reinterpret_cast<const bf16x8*>(ap), a1 = *reinterpret_cast<const bf16x8*>(ap + 32);
      f32x4 Dg = {0.f, 0.f, 0.f, 0.f}, Du = {0.f, 0.f, 0.f, 0.f};
      Dg = __builtin_amdgcn_mfma_f32_16x16x32_bf16(a0, GB[0][0], Dg, 0, 0, 0);
      Du = __builtin_amdgcn_mfma_f32_16x16x32_bf16(a0, GB[1][0], Du, 0, 0, 0);
      Dg = __builtin_amdgcn_mfma_f32_16x16x32_bf16(a1, GB[0][1], Dg, 0, 0, 0);
      Du = __builtin_amdgcn_mfma_f32_16x16x32_bf16(a1, GB[1][1], Du, 0, 0, 0);
      if (lane < 16) *reinterpret_cast<f32x4*>(lds + L_QP + 64 + (wave * 16 + lane) * 4) = f32x4{Dg[0], Dg[1], Du[0], Du[1]};
    }
    __syncthreads();
    if (tid < 64) {
      float g = 0.f, u = 0.f;
#pragma unroll
      for (int w = 0; w < NW; ++w) { g += lds[L_QP + 64 + (w * 16 + c) * 4 + (r & 1)]; u += lds[L_QP + 64 + (w * 16 + c) * 4 + 2 + (r & 1)]; }
      g = rbf(g); u = rbf(u);
      const float sg = rbf(g / (1.f + __expf(-g)));
      const u32 av = f2bf(sg * u);
      auto t = __builtin_amdgcn_permlane16_swap(av, av, false, false);
      const u32 pk = t[0] | (t[1] << 16);
      st_gran(ws + WS_AG + r * FF + rcol0 + c, tag, pk);
      st_gran(ws + WS_AG + (r + 4) * FF + rcol0 + c, tag, pk);
    }
    STAMP(8);
  } else if (role == 0) {
    const u64* ap = ws + WS_AG + xcd * FF + tid;
    const u64* pp[5] = {ap, ap + 512, ap + 1024, tid < 256 ? ap + 1536 : ap, ws + WS_HG + xcd * 512 + rcol0 + (tid & 7)};
    u32 v[5];
    poll_gran<5>(pp, tag, v);
    STAMP(7);
    f32x2 a0[4], a1[4];
#pragma unroll
    for (int k = 0; k < 4; ++k) { a0[k] = f32x2{0.f, 0.f}; a1[k] = f32x2{0.f, 0.f}; }
#pragma unroll
    for (int i = 0; i < 4; ++i) {
      float l0 = lo2f(v[i]), l1 = hi2f(v[i]);
      if (i == 3 && tid >= 256) { l0 = 0.f; l1 = 0.f; }
      fma8(a0, l0, RW[i]);
      fma8(a1, l1, RW[i]);
    }
    float y[8];
#pragma unroll
    for (int k = 0; k < 4; ++k) { y[2 * k] = red32(a0[k][0], a1[k][0]); y[2 * k + 1] = red32(a0[k][1], a1[k][1]); }
#pragma unroll
    for (int k = 0; k < 4; ++k) y[k] = red16(y[k], y[k + 4]);
#pragma unroll
    for (int k = 0; k < 2; ++k) y[k] = redd<ROR8>(y[k], y[k + 2], c & 8);
    y[0] = redd<XOR7>(y[0], y[1], c & 4);
    y[0] += dpp<XOR2>(y[0]);
    y[0] += dpp<XOR1>(y[0]);
    if ((lane & 3) == 0) red[wave * 16 + (lane >> 2)] = y[0];
    __syncthreads();
    if (tid < 16) {
      float s = 0.f;
#pragma unroll
      for (int w = 0; w < NW; ++w) s += red[w * 16 + tid];
      float hh = (tid < 8) ? lo2f(v[4]) : hi2f(v[4]);
      out[(tid >> 3) * DM + rcol0 + (tid & 7)] = (u16)f2bf(hh + rbf(s));
    }
    STAMP(8);
    if (bid == 0 && tid == 0) __hip_atomic_store((u32*)(ws + WS_E), tag, __ATOMIC_RELAXED, __HIP_MEMORY_SCOPE_AGENT);
  }
}

extern "C" void kernel_launch(void* const* d_in, const int* in_sizes, int n_in,
                              void* d_out, int out_size, void* d_ws, size_t ws_size,
                              hipStream_t stream) {
  fused_kernel<<<256, NTHR, 0, stream>>>((const u16*)d_in[0], (const u16*)d_in[1], (const u16*)d_in[2], (const u16*)d_in[3],
                                         (const u16*)d_in[4], (const u16*)d_in[6], (const u16*)d_in[7], (const u16*)d_in[8],
                                         (u16*)d_out, (u64*)d_ws);
}
```

```cpp
#include <hip/hip_runtime.h>
#include <hip/hip_bf16.h>
#include <stdint.h>

typedef unsigned int u32;
typedef unsigned short u16;
typedef unsigned long long u64;
typedef __attribute__((ext_vector_type(4))) unsigned u32x4;
typedef __attribute__((ext_vector_type(4))) float f32x4;
typedef __attribute__((ext_vector_type(2))) float f32x2;
typedef __attribute__((ext_vector_type(2))) __bf16 bf16x2;
typedef __attribute__((ext_vector_type(4))) __bf16 bf16x4;
typedef __attribute__((ext_vector_type(8))) __bf16 bf16x8;

constexpr int NB = 2, DM = 512, NH = 32, SK = 16384, DH = 128, FF = 1792;
constexpr int NSPLIT = 4;
constexpr int NW = 8;
constexpr int NTHR = NW * 64;
constexpr int KPW = SK / NSPLIT / NW;
constexpr int TK = 32;
constexpr int NT = KPW / TK;
constexpr float SCALE = 0.08838834764831845f;
constexpr float LOG2E = 1.4426950408889634f;
constexpr unsigned SPIN_CAP = 1u << 22;

constexpr size_t WS_E = 0;
constexpr size_t WS_QG = 64;
constexpr size_t WS_ML = WS_QG + 64 * 64;
constexpr size_t WS_PART = WS_ML + 64 * 64;
constexpr size_t WS_HG = WS_PART + 2 * 128 * 512;
constexpr size_t WS_AG = WS_HG + 8 * 512;

__device__ __forceinline__ float bf2f(u32 v) { return __uint_as_float(v << 16); }
__device__ __forceinline__ float lo2f(u32 v) { return __uint_as_float(v << 16); }
__device__ __forceinline__ float hi2f(u32 v) { return __uint_as_float(v & 0xffff0000u); }
__device__ __forceinline__ float rbf(float x) { return (float)(__bf16)x; }
__device__ __forceinline__ u32 f2bf(float x) { __bf16 b = (__bf16)x; return (u32)__builtin_bit_cast(u16, b); }
__device__ __forceinline__ float dot2(u32 a, u32 b, float acc) {
  return __builtin_amdgcn_fdot2_f32_bf16(__builtin_bit_cast(bf16x2, a), __builtin_bit_cast(bf16x2, b), acc, false);
}
template <int CTRL> __device__ __forceinline__ float dpp(float x) {
  return __builtin_bit_cast(float, __builtin_amdgcn_mov_dpp(__builtin_bit_cast(int, x), CTRL, 0xf, 0xf, true));
}
constexpr int XOR1 = 0xB1, XOR2 = 0x4E, XOR7 = 0x141, ROR8 = 0x128, ROR4 = 0x124, ROR2 = 0x122, ROR1 = 0x121;
__device__ __forceinline__ float red32(float a, float b) {
  auto t = __builtin_amdgcn_permlane32_swap(__float_as_uint(a), __float_as_uint(b), false, false);
  return __uint_as_float(t[0]) + __uint_as_float(t[1]);
}
__device__ __forceinline__ float red16(float a, float b) {
  auto t = __builtin_amdgcn_permlane16_swap(__float_as_uint(a), __float_as_uint(b), false, false);
  return __uint_as_float(t[0]) + __uint_as_float(t[1]);
}
template <int CTRL> __device__ __forceinline__ float redd(float a, float b, bool hi) {
  const float t1 = a + dpp<CTRL>(a), t2 = b + dpp<CTRL>(b);
  return hi ? t2 : t1;
}
__device__ __forceinline__ float xrow_sum(float x) { x = red16(x, x); return red32(x, x); }
__device__ __forceinline__ float xrow_max(float x) {
  auto s = __builtin_amdgcn_permlane16_swap(__float_as_uint(x), __float_as_uint(x), false, false);
  x = fmaxf(__uint_as_float(s[0]), __uint_as_float(s[1]));
  auto t = __builtin_amdgcn_permlane32_swap(__float_as_uint(x), __float_as_uint(x), false, false);
  return fmaxf(__uint_as_float(t[0]), __uint_as_float(t[1]));
}
__device__ __forceinline__ float wave_sum(float x) {
  x += dpp<ROR8>(x); x += dpp<ROR4>(x); x += dpp<ROR2>(x); x += dpp<ROR1>(x);
  return xrow_sum(x);
}
__device__ __forceinline__ float wave_max(float x) {
  x = fmaxf(x, dpp<ROR8>(x)); x = fmaxf(x, dpp<ROR4>(x)); x = fmaxf(x, dpp<ROR2>(x)); x = fmaxf(x, dpp<ROR1>(x));
  return xrow_max(x);
}
__device__ __forceinline__ void st_gran(u64* p, u32 tag, u32 val) {
  __hip_atomic_store(p, ((u64)tag << 32) | (u64)val, __ATOMIC_RELAXED, __HIP_MEMORY_SCOPE_AGENT);
}
__device__ __forceinline__ u64 ld_gran(const u64* p) {
  return __hip_atomic_load(p, __ATOMIC_RELAXED, __HIP_MEMORY_SCOPE_AGENT);
}
template <int N> __device__ __forceinline__ void poll_gran(const u64* const (&p)[N], u32 tag, u32 (&v)[N]) {
  for (unsigned spins = 0;; ++spins) {
    bool ok = true;
#pragma unroll
    for (int k = 0; k < N; ++k) { u64 g = ld_gran(p[k]); v[k] = (u32)g; ok &= ((u32)(g >> 32) == tag); }
    if (__all(ok) || spins > SPIN_CAP) break;
    __builtin_amdgcn_s_sleep(1);
  }
}
__device__ __forceinline__ float fdiv(float a, float d, float r) { const float q = a * r; return fmaf(fmaf(-q, d, a), r, q); }
__device__ __forceinline__ void fma8(f32x2 (&a)[4], float s, u32x4 w) {
  const f32x2 ss = {s, s};
#pragma unroll
  for (int k = 0; k < 4; ++k) { const f32x2 v = {lo2f(w[k]), hi2f(w[k])}; a[k] = __builtin_elementwise_fma(ss, v, a[k]); }
  asm volatile("" : "+v"(a[0]), "+v"(a[1]), "+v"(a[2]), "+v"(a[3]));
}
__device__ __forceinline__ void wave_ln_stats2(const float (&x)[8], const float (&y)[8], float& m0, float& d0, float& m1, float& d1) {
  float s0 = ((x[0] + x[1]) + (x[2] + x[3])) + ((x[4] + x[5]) + (x[6] + x[7]));
  float s1 = ((y[0] + y[1]) + (y[2] + y[3])) + ((y[4] + y[5]) + (y[6] + y[7]));
  s0 = wave_sum(s0) * (1.f / 512.f); s1 = wave_sum(s1) * (1.f / 512.f);
  float v0 = 0.f, v1 = 0.f;
#pragma unroll
  for (int k = 0; k < 8; ++k) { v0 += (x[k] - s0) * (x[k] - s0); v1 += (y[k] - s1) * (y[k] - s1); }
  v0 = wave_sum(v0) * (1.f / 512.f); v1 = wave_sum(v1) * (1.f / 512.f);
  m0 = rbf(s0); m1 = rbf(s1);
  d0 = rbf(sqrtf(rbf(rbf(v0) + 1e-5f))); d1 = rbf(sqrtf(rbf(rbf(v1) + 1e-5f)));
}

#define SB() __builtin_amdgcn_sched_barrier(0)
#define LAS __attribute__((address_space(3)))
#define FENCE() do { asm volatile("" ::: "memory"); __builtin_amdgcn_sched_barrier(0); } while (0)
__device__ __forceinline__ void glds16(const void* gsrc, unsigned lds_dst) {
  unsigned keep;
  asm volatile("s_mov_b32 %0, m0\n\ts_mov_b32 m0, %2\n\ts_nop 0\n\tglobal_load_lds_dwordx4 %1, off\n\ts_mov_b32 m0, %0" : "=&s"(keep) : "v"(gsrc), "s"(lds_dst) : "memory");
}
#define STAMP(i_) do {} while (0)
constexpr int L_SC = 0;
constexpr int L_QP = L_SC + NW * 512;
constexpr int L_RED = L_QP + NW * 128;
constexpr int L_OI = L_RED + 256;
constexpr int L_WO = L_OI + 192;
constexpr int L_TOT = L_WO + NW * 16 * 256;

__global__ void __launch_bounds__(NTHR, 2) fused_kernel(const u16* __restrict__ x, const u16* __restrict__ kc, const u16* __restrict__ vc,
    const u16* __restrict__ wq, const u16* __restrict__ wo, const u16* __restrict__ wg, const u16* __restrict__ wu,
    const u16* __restrict__ wd, u16* __restrict__ out, u64* __restrict__ ws) {
  __shared__ __attribute__((aligned(16))) float lds[L_TOT];
  const int tid = threadIdx.x, lane = tid & 63, r = lane >> 4, c = lane & 15;
  const int wave = __builtin_amdgcn_readfirstlane(tid >> 6);
  const int bid = blockIdx.x, xcd = bid & 7, idx = bid >> 3;
  const int head = xcd * 4 + (idx & 3), g8 = idx >> 2, b = g8 & 1, split = g8 >> 1;
  const int bh = b * NH + head;
  const int role = idx < 8 ? 0 : (idx < 16 ? 1 : (idx < 30 ? 2 : 3));
  const int rcol0 = role == 0 ? 8 * (xcd * 8 + idx) : (role == 2 ? 16 * (xcd * 14 + idx - 16) : 8 * (xcd * 8 + ((idx - 8) & 7)));
  float* const sc = lds + L_SC + wave * 512;
  float* const red = lds + L_RED;

  STAMP(0);
  const u32 epoch = (u32)__hip_atomic_load((const u32*)(ws + WS_E), __ATOMIC_RELAXED, __HIP_MEMORY_SCOPE_AGENT);
  const u32x4 xa = *reinterpret_cast<const u32x4*>(x + 8 * lane), xb = *reinterpret_cast<const u32x4*>(x + DM + 8 * lane);
  const u32 xo0 = x[tid], xo1 = x[DM + tid];
  const u32x4 wq0 = *reinterpret_cast<const u32x4*>(wq + (size_t)tid * 4096 + head * DH + 16 * g8);
  const u32x4 wq1 = *reinterpret_cast<const u32x4*>(wq + (size_t)tid * 4096 + head * DH + 16 * g8 + 8);
  const size_t base = ((size_t)bh * SK + (size_t)split * (SK / NSPLIT) + (size_t)wave * TK) * DH + (size_t)lane * 8;
  const u16* kb = kc + base;
  const u16* vb = vc + base;
  u32x4 A[8], Bf[8];
#define ISSUE(BUF, j_) do { const u16* s_ = ((j_) < NT ? kb : vb) + (size_t)((j_) & (NT - 1)) * (NW * TK * DH); \
    _Pragma("unroll") for (int t_ = 0; t_ < 8; ++t_) BUF[t_] = __builtin_nontemporal_load(reinterpret_cast<const u32x4*>(s_ + t_ * 4 * DH)); } while (0)
  SB();
  __builtin_amdgcn_s_barrier();
  SB();
  ISSUE(A, 0);
  SB();

  const u32 tag = epoch + 1u;
  {
    float xf[8], yf[8], m0, d0, m1, d1;
#pragma unroll
    for (int k = 0; k < 4; ++k) { xf[2 * k] = lo2f(xa[k]); xf[2 * k + 1] = hi2f(xa[k]); yf[2 * k] = lo2f(xb[k]); yf[2 * k + 1] = hi2f(xb[k]); }
    wave_ln_stats2(xf, yf, m0, d0, m1, d1);
    const float l0 = rbf(rbf(bf2f(xo0) - m0) / d0), l1 = rbf(rbf(bf2f(xo1) - m1) / d1);
    STAMP(9);
    float y[32];
#pragma unroll
    for (int k = 0; k < 4; ++k) {
      y[2 * k] = l0 * lo2f(wq0[k]); y[2 * k + 1] = l0 * hi2f(wq0[k]); y[8 + 2 * k] = l0 * lo2f(wq1[k]); y[8 + 2 * k + 1] = l0 * hi2f(wq1[k]);
      y[16 + 2 * k] = l1 * lo2f(wq0[k]); y[16 + 2 * k + 1] = l1 * hi2f(wq0[k]); y[24 + 2 * k] = l1 * lo2f(wq1[k]); y[24 + 2 * k + 1] = l1 * hi2f(wq1[k]);
    }
    STAMP(10);
#pragma unroll
    for (int i = 0; i < 16; ++i) y[i] = red32(y[i], y[i + 16]);
#pragma unroll
    for (int i = 0; i < 8; ++i) y[i] = red16(y[i], y[i + 8]);
#pragma unroll
    for (int i = 0; i < 4; ++i) y[i] = redd<ROR8>(y[i], y[i + 4], c & 8);
#pragma unroll
    for (int i = 0; i < 2; ++i) y[i] = redd<XOR7>(y[i], y[i + 2], c & 4);
    y[0] = redd<XOR2>(y[0], y[1], c & 2);
    y[0] += dpp<XOR1>(y[0]);
    red[wave * 32 + (lane >> 1)] = y[0];
  }
  STAMP(11);
  __syncthreads();
  if (tid < 16) {
    float s0 = 0.f, s1 = 0.f;
#pragma unroll
    for (int w = 0; w < NW; ++w) { f32x2 t = *reinterpret_cast<const f32x2*>(red + w * 32 + 2 * tid); s0 += t[0]; s1 += t[1]; }
    st_gran(ws + WS_QG + ((size_t)(tid >> 3) * NH + head) * 64 + 8 * g8 + (tid & 7), tag, f2bf(s0) | (f2bf(s1) << 16));
  }
  STAMP(12);
  SB();
  ISSUE(Bf, 1);
  SB();
  u64* const part_p = ws + WS_PART + ((size_t)b * 128 + head * 4 + split) * 512 + wave * 64 + c;
  u64* const ml_p = ws + WS_ML + (size_t)bh * 64 + (split * NW + wave) * 2;
  if (lane < 16) {
#pragma unroll
    for (int nt = 0; nt < 4; ++nt) st_gran(part_p + 16 * nt, epoch, 0u);
  }
  if (tid < 64) lds[L_OI + 128 + tid] = 0.f;
  if (lane < 2) st_gran(ml_p + lane, epoch, 0u);
  if (role == 1 && tid < 64) st_gran(ws + WS_HG + (tid >> 3) * 512 + rcol0 + (tid & 7), epoch, 0u);
  if (role == 2 && tid < 64) { st_gran(ws + WS_AG + (tid >> 4) * FF + rcol0 + c, epoch, 0u); st_gran(ws + WS_AG + ((tid >> 4) + 4) * FF + rcol0 + c, epoch, 0u); }
  u32 qp[4];
  {
    const u64* q0 = ws + WS_QG + (size_t)bh * 64 + 4 * c;
    const u64* pp[4] = {q0, q0 + 1, q0 + 2, q0 + 3};
    poll_gran<4>(pp, tag, qp);
  }

  STAMP(1);
  float mx = -INFINITY;
#define COMPUTE_K(BUF, j_) do { float x_[8]; \
    _Pragma("unroll") for (int t_ = 0; t_ < 8; ++t_) { float d_ = dot2(BUF[t_][0], qp[0], 0.f); d_ = dot2(BUF[t_][1], qp[1], d_); \
      d_ = dot2(BUF[t_][2], qp[2], d_); x_[t_] = dot2(BUF[t_][3], qp[3], d_); } \
    _Pragma("unroll") for (int h_ = 0; h_ < 4; ++h_) x_[h_] = redd<ROR8>(x_[h_], x_[h_ + 4], c & 8); \
    _Pragma("unroll") for (int h_ = 0; h_ < 2; ++h_) x_[h_] = redd<XOR7>(x_[h_], x_[h_ + 2], c & 4); \
    x_[0] = redd<XOR2>(x_[0], x_[1], c & 2); \
    x_[0] += dpp<XOR1>(x_[0]); \
    float s_ = rbf(rbf(x_[0]) * SCALE) * LOG2E; \
    mx = fmaxf(mx, s_); \
    sc[(j_) * 32 + r * 8 + (c >> 1)] = s_; } while (0)

  SB();
  COMPUTE_K(A, 0); SB(); ISSUE(A, 2); SB();
  COMPUTE_K(Bf, 1); SB(); ISSUE(Bf, 3); SB();
  u32x4 RW[4];
  {
    const unsigned wdst = __builtin_amdgcn_readfirstlane((unsigned)(size_t)((LAS float*)lds + L_WO) + (unsigned)wave * 16384u);
    const u16* p = wo + (size_t)(head * DH + 16 * wave) * DM;
#pragma unroll
    for (int i = 0; i < 16; ++i) {
      const int sw = 2 * ((i & 3) | (((i >> 3) & 1) << 2));
      glds16(p + (size_t)i * DM + 8 * (lane ^ sw), wdst + i * 1024);
    }
  }
  {
    const u16* p; size_t st;
    if (role == 0) {
      p = wd + (size_t)tid * DM + rcol0; st = (size_t)512 * DM;
    } else {
      p = wd; st = 0;
    }
#pragma unroll
    for (int i = 0; i < 3; ++i) RW[i] = *reinterpret_cast<const u32x4*>(p + (size_t)i * st);
    RW[3] = *reinterpret_cast<const u32x4*>(p + ((role == 0 && tid >= 256) ? (size_t)0 : 3 * st));
  }
  u16 GW[2][2][8];
  {
    const size_t goff = role == 2 ? (size_t)(64 * wave + 8 * r) * FF + rcol0 + c : (size_t)0;
    const size_t gst = role == 2 ? (size_t)FF : (size_t)0;
#pragma unroll
    for (int ks = 0; ks < 2; ++ks)
#pragma unroll
      for (int j = 0; j < 8; ++j) { GW[0][ks][j] = wg[goff + (32 * ks + j) * gst]; GW[1][ks][j] = wu[goff + (32 * ks + j) * gst]; }
  }
  SB();
#pragma unroll 1
  for (int j = 2; j < NT; j += 2) {
    COMPUTE_K(A, j); SB(); __builtin_amdgcn_s_barrier(); SB(); ISSUE(A, j + 2); SB();
    COMPUTE_K(Bf, j + 1); SB(); __builtin_amdgcn_s_barrier(); SB(); ISSUE(Bf, j + 3); SB();
  }
  STAMP(2);
  const float mw = wave_max(mx);
  {
    f32x4 s0 = *reinterpret_cast<const f32x4*>(sc + 8 * lane), s1 = *reinterpret_cast<const f32x4*>(sc + 8 * lane + 4);
    float l = 0.f;
#pragma unroll
    for (int k = 0; k < 4; ++k) { s0[k] = __builtin_amdgcn_exp2f(s0[k] - mw); s1[k] = __builtin_amdgcn_exp2f(s1[k] - mw); l += s0[k] + s1[k]; }
    *reinterpret_cast<f32x4*>(sc + 8 * lane) = s0;
    *reinterpret_cast<f32x4*>(sc + 8 * lane + 4) = s1;
    const float lw = wave_sum(l);
    if (lane < 2) st_gran(ml_p + lane, tag, __float_as_uint(lane == 0 ? mw : lw));
  }
  STAMP(3);

#define COMPUTE_V(BUF, j_) do { \
    f32x4 p0_ = *reinterpret_cast<const f32x4*>(sc + (j_) * 32 + r * 8), p1_ = *reinterpret_cast<const f32x4*>(sc + (j_) * 32 + r * 8 + 4); \
    _Pragma("unroll") for (int t_ = 0; t_ < 8; ++t_) { float p_ = t_ < 4 ? p0_[t_ & 3] : p1_[t_ & 3]; \
      asm volatile("" : "+v"(BUF[t_]));    \
      const f32x2 pp_ = {p_, p_}; \
      _Pragma("unroll") for (int k_ = 0; k_ < 4; ++k_) { \
        const f32x2 v_ = {lo2f(BUF[t_][k_]), hi2f(BUF[t_][k_])}; \
        acc2[k_] = __builtin_elementwise_fma(pp_, v_, acc2[k_]); } \
      if (t_ & 1) SB(); } } while (0)
  f32x2 acc2[4];
#pragma unroll
  for (int k = 0; k < 4; ++k) acc2[k] = f32x2{0.f, 0.f};
#pragma unroll 1
  for (int j = 0; j < NT - 2; j += 2) {
    COMPUTE_V(A, j); SB(); __builtin_amdgcn_s_barrier(); SB(); ISSUE(A, NT + j + 2); SB();
    COMPUTE_V(Bf, j + 1); SB(); __builtin_amdgcn_s_barrier(); SB(); ISSUE(Bf, NT + j + 3); SB();
  }
  bf16x8 WB[4][4];
  {
    const LAS char* wbase = (const LAS char*)((LAS float*)lds + L_WO);
    const int q_ = c >> 2, p_ = c & 3;
#pragma unroll
    for (int ks = 0; ks < 4; ++ks)
#pragma unroll
      for (int nt = 0; nt < 4; ++nt) {
        const int sw = 2 * (q_ | ((r & 1) << 2));
        const int ch = (2 * (4 * wave + nt) + (p_ >> 1)) ^ sw;
        const int row = 32 * ks + 8 * r + q_;
        const bf16x4 lo = __builtin_amdgcn_ds_read_tr16_b64_v4bf16((LAS bf16x4*)(wbase + row * 1024 + ch * 16 + 8 * (p_ & 1)));
        const bf16x4 hi = __builtin_amdgcn_ds_read_tr16_b64_v4bf16((LAS bf16x4*)(wbase + (row + 4) * 1024 + ch * 16 + 8 * (p_ & 1)));
        WB[ks][nt] = __builtin_shufflevector(lo, hi, 0, 1, 2, 3, 4, 5, 6, 7);
      }
  }
  float fnorm;
  {
    const u64* pp[1] = {ws + WS_ML + (size_t)bh * 64 + lane};
    u32 v[1];
    poll_gran<1>(pp, tag, v);
    float val = __uint_as_float(v[0]);
    float other = dpp<XOR1>(val);
    float mj = (lane & 1) ? other : val, lj = (lane & 1) ? val : other;
    float M = wave_max(mj);
    float L = wave_sum((lane & 1) ? 0.f : __builtin_amdgcn_exp2f(mj - M) * lj);
    fnorm = __builtin_amdgcn_exp2f(mw - M) / L;
  }
  SB();
  COMPUTE_V(A, NT - 2);
  COMPUTE_V(Bf, NT - 1);
  STAMP(4);
  {
    float t0 = red16(acc2[0][0], acc2[1][0]), t1 = red16(acc2[0][1], acc2[1][1]);
    float t2 = red16(acc2[2][0], acc2[3][0]), t3 = red16(acc2[2][1], acc2[3][1]);
    float u0 = red32(t0, t2), u1 = red32(t1, t3);
    *reinterpret_cast<f32x2*>(lds + L_QP + wave * 128 + 8 * c + 2 * r) = f32x2{u0 * fnorm, u1 * fnorm};
  }
  __syncthreads();
  STAMP(5);
  if (tid < 128) {
    float o = 0.f;
#pragma unroll
    for (int w = 0; w < NW; ++w) o += lds[L_QP + w * 128 + tid];
    const u32 hi = f2bf(o), lo = f2bf(o - bf2f(hi));
    u16* oi = reinterpret_cast<u16*>(lds + L_OI);
    oi[tid] = (u16)hi; oi[128 + tid] = (u16)lo;
  }
  __syncthreads();
  {
    const u16* oi = reinterpret_cast<const u16*>(lds + L_OI) + (c < 2 ? c : 2) * 128 + 8 * r;
    f32x4 D[4];
#pragma unroll
    for (int nt = 0; nt < 4; ++nt) D[nt] = f32x4{0.f, 0.f, 0.f, 0.f};
#pragma unroll
    for (int ks = 0; ks < 4; ++ks) {
      const bf16x8 af = *reinterpret_cast<const bf16x8*>(oi + 32 * ks);
#pragma unroll
      for (int nt = 0; nt < 4; ++nt) D[nt] = __builtin_amdgcn_mfma_f32_16x16x32_bf16(af, WB[ks][nt], D[nt], 0, 0, 0);
    }
    if (lane < 16) {
#pragma unroll
      for (int nt = 0; nt < 4; ++nt) st_gran(part_p + 16 * nt, tag, __float_as_uint(D[nt][0] + D[nt][1]));
    }
  }
  STAMP(6);

  if (role == 1) {
    const int seg = tid >> 1, half = tid & 1;
    const u64* p0 = ws + WS_PART + (size_t)seg * 512 + rcol0 + 4 * half;
    const u64* pp[4] = {p0, p0 + 1, p0 + 2, p0 + 3};
    const float xr0 = bf2f(x[rcol0 + (tid & 7)]), xr1 = bf2f(x[DM + rcol0 + (tid & 7)]);
    u32 v[4];
    poll_gran<4>(pp, tag, v);
    STAMP(7);
    float f0 = red32(__uint_as_float(v[0]), __uint_as_float(v[2])), f1 = red32(__uint_as_float(v[1]), __uint_as_float(v[3]));
    f0 = red16(f0, f1);
    f0 += dpp<ROR8>(f0); f0 += dpp<ROR4>(f0); f0 += dpp<XOR2>(f0);
    if ((lane & 14) == 0) red[wave * 8 + 4 * half + (lane >> 4)] = f0;
    __syncthreads();
    if (tid < 64) {
      const int e = tid & 7;
      float s0 = (red[e] + red[8 + e]) + (red[16 + e] + red[24 + e]);
      float s1 = (red[32 + e] + red[40 + e]) + (red[48 + e] + red[56 + e]);
      u32 h0 = f2bf(xr0 + rbf(s0));
      u32 h1 = f2bf(xr1 + rbf(s1));
      st_gran(ws + WS_HG + (tid >> 3) * 512 + rcol0 + e, tag, h0 | (h1 << 16));
    }
    STAMP(8);
  } else if (role == 2) {
    float* const p_st = lds + L_QP;
    u16* const p_ln = reinterpret_cast<u16*>(lds + L_SC);
    bf16x8 GB[2][2];
#pragma unroll
    for (int mt = 0; mt < 2; ++mt)
#pragma unroll
      for (int ks = 0; ks < 2; ++ks) {
        u32x4 t;
#pragma unroll
        for (int j = 0; j < 4; ++j) t[j] = (u32)GW[mt][ks][2 * j] | ((u32)GW[mt][ks][2 * j + 1] << 16);
        GB[mt][ks] = __builtin_bit_cast(bf16x8, t);
      }
    p_ln[1024 + tid] = 0;
    float h0, h1;
    {
      const u64* pp[1] = {ws + WS_HG + xcd * 512 + tid};
      u32 v[1];
      poll_gran<1>(pp, tag, v);
      STAMP(7);
      h0 = lo2f(v[0]); h1 = hi2f(v[0]);
      const float mu0 = wave_sum(h0) * (1.f / 64.f), mu1 = wave_sum(h1) * (1.f / 64.f);
      const float q0 = wave_sum((h0 - mu0) * (h0 - mu0)), q1 = wave_sum((h1 - mu1) * (h1 - mu1));
      if (lane == 0) *reinterpret_cast<f32x4*>(p_st + 4 * wave) = f32x4{mu0, q0, mu1, q1};
    }
    __syncthreads();
    {
      f32x4 st[8];
#pragma unroll
      for (int w = 0; w < NW; ++w) st[w] = *reinterpret_cast<const f32x4*>(p_st + 4 * w);
      float m0 = 0.f, m1 = 0.f, v0 = 0.f, v1 = 0.f;
#pragma unroll
      for (int w = 0; w < NW; ++w) { m0 += st[w][0]; m1 += st[w][2]; v0 += st[w][1]; v1 += st[w][3]; }
      m0 *= 0.125f; m1 *= 0.125f;
#pragma unroll
      for (int w = 0; w < NW; ++w) { v0 = fmaf(64.f * (st[w][0] - m0), st[w][0] - m0, v0); v1 = fmaf(64.f * (st[w][2] - m1), st[w][2] - m1, v1); }
      const float d0 = rbf(sqrtf(rbf(rbf(v0 * (1.f / 512.f)) + 1e-5f))), d1 = rbf(sqrtf(rbf(rbf(v1 * (1.f / 512.f)) + 1e-5f)));
      p_ln[tid] = (u16)f2bf(rbf(h0 - rbf(m0)) / d0);
      p_ln[512 + tid] = (u16)f2bf(rbf(h1 - rbf(m1)) / d1);
    }
    __syncthreads();
    {
      const u16* ap = p_ln + (c < 2 ? c : 2) * 512 + 64 * wave + 8 * r;
      const bf16x8 a0 = *reinterpret_cast<const bf16x8*>(ap), a1 = *reinterpret_cast<const bf16x8*>(ap + 32);
      f32x4 Dg = {0.f, 0.f, 0.f, 0.f}, Du = {0.f, 0.f, 0.f, 0.f};
      Dg = __builtin_amdgcn_mfma_f32_16x16x32_bf16(a0, GB[0][0], Dg, 0, 0, 0);
      Du = __builtin_amdgcn_mfma_f32_16x16x32_bf16(a0, GB[1][0], Du, 0, 0, 0);
      Dg = __builtin_amdgcn_mfma_f32_16x16x32_bf16(a1, GB[0][1], Dg, 0, 0, 0);
      Du = __builtin_amdgcn_mfma_f32_16x16x32_bf16(a1, GB[1][1], Du, 0, 0, 0);
      if (lane < 16) *reinterpret_cast<f32x4*>(lds + L_QP + 64 + (wave * 16 + lane) * 4) = f32x4{Dg[0], Dg[1], Du[0], Du[1]};
    }
    __syncthreads();
    if (tid < 64) {
      float g = 0.f, u = 0.f;
#pragma unroll
      for (int w = 0; w < NW; ++w) { g += lds[L_QP + 64 + (w * 16 + c) * 4 + (r & 1)]; u += lds[L_QP + 64 + (w * 16 + c) * 4 + 2 + (r & 1)]; }
      g = rbf(g); u = rbf(u);
      const float sg = rbf(g / (1.f + __expf(-g)));
      const u32 av = f2bf(sg * u);
      auto t = __builtin_amdgcn_permlane16_swap(av, av, false, false);
      const u32 pk = t[0] | (t[1] << 16);
      st_gran(ws + WS_AG + r * FF + rcol0 + c, tag, pk);
      st_gran(ws + WS_AG + (r + 4) * FF + rcol0 + c, tag, pk);
    }
    STAMP(8);
  } else if (role == 0) {
    const u64* ap = ws + WS_AG + xcd * FF + tid;
    const u64* pp[5] = {ap, ap + 512, ap + 1024, tid < 256 ? ap + 1536 : ap, ws + WS_HG + xcd * 512 + rcol0 + (tid & 7)};
    u32 v[5];
    poll_gran<5>(pp, tag, v);
    STAMP(7);
    f32x2 a0[4], a1[4];
#pragma unroll
    for (int k = 0; k < 4; ++k) { a0[k] = f32x2{0.f, 0.f}; a1[k] = f32x2{0.f, 0.f}; }
#pragma unroll
    for (int i = 0; i < 4; ++i) {
      float l0 = lo2f(v[i]), l1 = hi2f(v[i]);
      if (i == 3 && tid >= 256) { l0 = 0.f; l1 = 0.f; }
      fma8(a0, l0, RW[i]);
      fma8(a1, l1, RW[i]);
    }
    float y[8];
#pragma unroll
    for (int k = 0; k < 4; ++k) { y[2 * k] = red32(a0[k][0], a1[k][0]); y[2 * k + 1] = red32(a0[k][1], a1[k][1]); }
#pragma unroll
    for (int k = 0; k < 4; ++k) y[k] = red16(y[k], y[k + 4]);
#pragma unroll
    for (int k = 0; k < 2; ++k) y[k] = redd<ROR8>(y[k], y[k + 2], c & 8);
    y[0] = redd<XOR7>(y[0], y[1], c & 4);
    y[0] += dpp<XOR2>(y[0]);
    y[0] += dpp<XOR1>(y[0]);
    if ((lane & 3) == 0) red[wave * 16 + (lane >> 2)] = y[0];
    __syncthreads();
    if (tid < 16) {
      float s = 0.f;
#pragma unroll
      for (int w = 0; w < NW; ++w) s += red[w * 16 + tid];
      float hh = (tid < 8) ? lo2f(v[4]) : hi2f(v[4]);
      out[(tid >> 3) * DM + rcol0 + (tid & 7)] = (u16)f2bf(hh + rbf(s));
    }
    STAMP(8);
    if (bid == 0 && tid == 0) __hip_atomic_store((u32*)(ws + WS_E), tag, __ATOMIC_RELAXED, __HIP_MEMORY_SCOPE_AGENT);
  }
}

extern "C" void kernel_launch(void* const* d_in, const int* in_sizes, int n_in,
                              void* d_out, int out_size, void* d_ws, size_t ws_size,
                              hipStream_t stream) {
  fused_kernel<<<256, NTHR, 0, stream>>>((const u16*)d_in[0], (const u16*)d_in[1], (const u16*)d_in[2], (const u16*)d_in[3],
                                         (const u16*)d_in[4], (const u16*)d_in[6], (const u16*)d_in[7], (const u16*)d_in[8],
                                         (u16*)d_out, (u64*)d_ws);
}
```

```cpp
#include <hip/hip_runtime.h>
#include <hip/hip_bf16.h>
#include <stdint.h>

typedef unsigned int u32;
typedef unsigned short u16;
typedef unsigned long long u64;
typedef __attribute__((ext_vector_type(4))) unsigned u32x4;
typedef __attribute__((ext_vector_type(4))) float f32x4;
typedef __attribute__((ext_vector_type(2))) float f32x2;
typedef __attribute__((ext_vector_type(2))) __bf16 bf16x2;
typedef __attribute__((ext_vector_type(4))) __bf16 bf16x4;
typedef __attribute__((ext_vector_type(8))) __bf16 bf16x8;

constexpr int NB = 2, DM = 512, NH = 32, SK = 16384, DH = 128, FF = 1792;
constexpr int NSPLIT = 4;
constexpr int NW = 8;
constexpr int NTHR = NW * 64;
constexpr int KPW = SK / NSPLIT / NW;
constexpr int TK = 32;
constexpr int NT = KPW / TK;
constexpr float SCALE = 0.08838834764831845f;
constexpr float LOG2E = 1.4426950408889634f;
constexpr unsigned SPIN_CAP = 1u << 22;

constexpr size_t WS_E = 0;
constexpr size_t WS_QG = 64;
constexpr size_t WS_ML = WS_QG + 64 * 64;
constexpr size_t WS_PART = WS_ML + 64 * 64;
constexpr size_t WS_HG = WS_PART + 2 * 128 * 512;
constexpr size_t WS_AG = WS_HG + 8 * 512;

__device__ __forceinline__ float bf2f(u32 v) { return __uint_as_float(v << 16); }
__device__ __forceinline__ float lo2f(u32 v) { return __uint_as_float(v << 16); }
__device__ __forceinline__ float hi2f(u32 v) { return __uint_as_float(v & 0xffff0000u); }
__device__ __forceinline__ float rbf(float x) { return (float)(__bf16)x; }
__device__ __forceinline__ u32 f2bf(float x) { __bf16 b = (__bf16)x; return (u32)__builtin_bit_cast(u16, b); }
__device__ __forceinline__ float dot2(u32 a, u32 b, float acc) {
  return __builtin_amdgcn_fdot2_f32_bf16(__builtin_bit_cast(bf16x2, a), __builtin_bit_cast(bf16x2, b), acc, false);
}
template <int CTRL> __device__ __forceinline__ float dpp(float x) {
  return __builtin_bit_cast(float, __builtin_amdgcn_mov_dpp(__builtin_bit_cast(int, x), CTRL, 0xf, 0xf, true));
}
constexpr int XOR1 = 0xB1, XOR2 = 0x4E, XOR7 = 0x141, ROR8 = 0x128, ROR4 = 0x124, ROR2 = 0x122, ROR1 = 0x121;
__device__ __forceinline__ float red32(float a, float b) {
  auto t = __builtin_amdgcn_permlane32_swap(__float_as_uint(a), __float_as_uint(b), false, false);
  return __uint_as_float(t[0]) + __uint_as_float(t[1]);
}
__device__ __forceinline__ float red16(float a, float b) {
  auto t = __builtin_amdgcn_permlane16_swap(__float_as_uint(a), __float_as_uint(b), false, false);
  return __uint_as_float(t[0]) + __uint_as_float(t[1]);
}
template <int CTRL> __device__ __forceinline__ float redd(float a, float b, bool hi) {
  const float t1 = a + dpp<CTRL>(a), t2 = b + dpp<CTRL>(b);
  return hi ? t2 : t1;
}
__device__ __forceinline__ float xrow_sum(float x) { x = red16(x, x); return red32(x, x); }
__device__ __forceinline__ float xrow_max(float x) {
  auto s = __builtin_amdgcn_permlane16_swap(__float_as_uint(x), __float_as_uint(x), false, false);
  x = fmaxf(__uint_as_float(s[0]), __uint_as_float(s[1]));
  auto t = __builtin_amdgcn_permlane32_swap(__float_as_uint(x), __float_as_uint(x), false, false);
  return fmaxf(__uint_as_float(t[0]), __uint_as_float(t[1]));
}
__device__ __forceinline__ float wave_sum(float x) {
  x += dpp<ROR8>(x); x += dpp<ROR4>(x); x += dpp<ROR2>(x); x += dpp<ROR1>(x);
  return xrow_sum(x);
}
__device__ __forceinline__ float wave_max(float x) {
  x = fmaxf(x, dpp<ROR8>(x)); x = fmaxf(x, dpp<ROR4>(x)); x = fmaxf(x, dpp<ROR2>(x)); x = fmaxf(x, dpp<ROR1>(x));
  return xrow_max(x);
}
__device__ __forceinline__ void st_gran(u64* p, u32 tag, u32 val) {
  __hip_atomic_store(p, ((u64)tag << 32) | (u64)val, __ATOMIC_RELAXED, __HIP_MEMORY_SCOPE_AGENT);
}
__device__ __forceinline__ u64 ld_gran(const u64* p) {
  return __hip_atomic_load(p, __ATOMIC_RELAXED, __HIP_MEMORY_SCOPE_AGENT);
}
template <int N> __device__ __forceinline__ void poll_gran(const u64* const (&p)[N], u32 tag, u32 (&v)[N]) {
  for (unsigned spins = 0;; ++spins) {
    bool ok = true;
#pragma unroll
    for (int k = 0; k < N; ++k) { u64 g = ld_gran(p[k]); v[k] = (u32)g; ok &= ((u32)(g >> 32) == tag); }
    if (__all(ok) || spins > SPIN_CAP) break;
    __builtin_amdgcn_s_sleep(1);
  }
}
__device__ __forceinline__ float fdiv(float a, float d, float r) { const float q = a * r; return fmaf(fmaf(-q, d, a), r, q); }
__device__ __forceinline__ void fma8(f32x2 (&a)[4], float s, u32x4 w) {
  const f32x2 ss = {s, s};
#pragma unroll
  for (int k = 0; k < 4; ++k) { const f32x2 v = {lo2f(w[k]), hi2f(w[k])}; a[k] = __builtin_elementwise_fma(ss, v, a[k]); }
  asm volatile("" : "+v"(a[0]), "+v"(a[1]), "+v"(a[2]), "+v"(a[3]));
}
__device__ __forceinline__ void wave_ln_stats2(const float (&x)[8], const float (&y)[8], float& m0, float& d0, float& m1, float& d1) {
  float s0 = ((x[0] + x[1]) + (x[2] + x[3])) + ((x[4] + x[5]) + (x[6] + x[7]));
  float s1 = ((y[0] + y[1]) + (y[2] + y[3])) + ((y[4] + y[5]) + (y[6] + y[7]));
  s0 = wave_sum(s0) * (1.f / 512.f); s1 = wave_sum(s1) * (1.f / 512.f);
  float v0 = 0.f, v1 = 0.f;
#pragma unroll
  for (int k = 0; k < 8; ++k) { v0 += (x[k] - s0) * (x[k] - s0); v1 += (y[k] - s1) * (y[k] - s1); }
  v0 = wave_sum(v0) * (1.f / 512.f); v1 = wave_sum(v1) * (1.f / 512.f);
  m0 = rbf(s0); m1 = rbf(s1);
  d0 = rbf(sqrtf(rbf(rbf(v0) + 1e-5f))); d1 = rbf(sqrtf(rbf(rbf(v1) + 1e-5f)));
}

#define SB() __builtin_amdgcn_sched_barrier(0)
#define LAS __attribute__((address_space(3)))
#define FENCE() do { asm volatile("" ::: "memory"); __builtin_amdgcn_sched_barrier(0); } while (0)
__device__ __forceinline__ void glds16(const void* gsrc, unsigned lds_dst) {
  unsigned keep;
  asm volatile("s_mov_b32 %0, m0\n\ts_mov_b32 m0, %2\n\ts_nop 0\n\tglobal_load_lds_dwordx4 %1, off\n\ts_mov_b32 m0, %0" : "=&s"(keep) : "v"(gsrc), "s"(lds_dst) : "memory");
}
#define STAMP(i_) do {} while (0)
constexpr int L_SC = 0;
constexpr int L_QP = L_SC + NW * 512;
constexpr int L_RED = L_QP + NW * 128;
constexpr int L_OI = L_RED + 256;
constexpr int L_WO = L_OI + 192;
constexpr int L_TOT = L_WO + NW * 16 * 256;

__global__ void __launch_bounds__(NTHR, 2) fused_kernel(const u16* __restrict__ x, const u16* __restrict__ kc, const u16* __restrict__ vc,
    const u16* __restrict__ wq, const u16* __restrict__ wo, const u16* __restrict__ wg, const u16* __restrict__ wu,
    const u16* __restrict__ wd, u16* __restrict__ out, u64* __restrict__ ws) {
  __shared__ __attribute__((aligned(16))) float lds[L_TOT];
  const int tid = threadIdx.x, lane = tid & 63, r = lane >> 4, c = lane & 15;
  const int wave = __builtin_amdgcn_readfirstlane(tid >> 6);
  const int bid = blockIdx.x, xcd = bid & 7, idx = bid >> 3;
  const int head = xcd * 4 + (idx & 3), g8 = idx >> 2, b = g8 & 1, split = g8 >> 1;
  const int bh = b * NH + head;
  const int role = idx < 8 ? 0 : (idx < 16 ? 1 : (idx < 30 ? 2 : 3));
  const int rcol0 = role == 0 ? 8 * (xcd * 8 + idx) : (role == 2 ? 16 * (xcd * 14 + idx - 16) : 8 * (xcd * 8 + ((idx - 8) & 7)));
  float* const sc = lds + L_SC + wave * 512;
  float* const red = lds + L_RED;

  STAMP(0);
  const u32 epoch = (u32)__hip_atomic_load((const u32*)(ws + WS_E), __ATOMIC_RELAXED, __HIP_MEMORY_SCOPE_AGENT);
  const u32x4 xa = *reinterpret_cast<const u32x4*>(x + 8 * lane), xb = *reinterpret_cast<const u32x4*>(x + DM + 8 * lane);
  const u32 xo0 = x[tid], xo1 = x[DM + tid];
  const u32x4 wq0 = *reinterpret_cast<const u32x4*>(wq + (size_t)tid * 4096 + head * DH + 16 * g8);
  const u32x4 wq1 = *reinterpret_cast<const u32x4*>(wq + (size_t)tid * 4096 + head * DH + 16 * g8 + 8);
  const size_t base = ((size_t)bh * SK + (size_t)split * (SK / NSPLIT) + (size_t)wave * TK) * DH + (size_t)lane * 8;
  const u16* kb = kc + base;
  const u16* vb = vc + base;
  const int rot = head & (NT - 1);
  u32x4 A[8], Bf[8];
#define ISSUE(BUF, j_) do { const u16* s_ = ((j_) < NT ? kb : vb) + (size_t)(((j_) + rot) & (NT - 1)) * (NW * TK * DH); \
    _Pragma("unroll") for (int t_ = 0; t_ < 8; ++t_) BUF[t_] = __builtin_nontemporal_load(reinterpret_cast<const u32x4*>(s_ + t_ * 4 * DH)); } while (0)
  SB();
  __builtin_amdgcn_s_barrier();
  SB();
  ISSUE(A, 0);
  SB();

  const u32 tag = epoch + 1u;
  {
    float xf[8], yf[8], m0, d0, m1, d1;
#pragma unroll
    for (int k = 0; k < 4; ++k) { xf[2 * k] = lo2f(xa[k]); xf[2 * k + 1] = hi2f(xa[k]); yf[2 * k] = lo2f(xb[k]); yf[2 * k + 1] = hi2f(xb[k]); }
    wave_ln_stats2(xf, yf, m0, d0, m1, d1);
    const float l0 = rbf(rbf(bf2f(xo0) - m0) / d0), l1 = rbf(rbf(bf2f(xo1) - m1) / d1);
    STAMP(9);
    float y[32];
#pragma unroll
    for (int k = 0; k < 4; ++k) {
      y[2 * k] = l0 * lo2f(wq0[k]); y[2 * k + 1] = l0 * hi2f(wq0[k]); y[8 + 2 * k] = l0 * lo2f(wq1[k]); y[8 + 2 * k + 1] = l0 * hi2f(wq1[k]);
      y[16 + 2 * k] = l1 * lo2f(wq0[k]); y[16 + 2 * k + 1] = l1 * hi2f(wq0[k]); y[24 + 2 * k] = l1 * lo2f(wq1[k]); y[24 + 2 * k + 1] = l1 * hi2f(wq1[k]);
    }
    STAMP(10);
#pragma unroll
    for (int i = 0; i < 16; ++i) y[i] = red32(y[i], y[i + 16]);
#pragma unroll
    for (int i = 0; i < 8; ++i) y[i] = red16(y[i], y[i + 8]);
#pragma unroll
    for (int i = 0; i < 4; ++i) y[i] = redd<ROR8>(y[i], y[i + 4], c & 8);
#pragma unroll
    for (int i = 0; i < 2; ++i) y[i] = redd<XOR7>(y[i], y[i + 2], c & 4);
    y[0] = redd<XOR2>(y[0], y[1], c & 2);
    y[0] += dpp<XOR1>(y[0]);
    red[wave * 32 + (lane >> 1)] = y[0];
  }
  STAMP(11);
  __syncthreads();
  if (tid < 16) {
    float s0 = 0.f, s1 = 0.f;
#pragma unroll
    for (int w = 0; w < NW; ++w) { f32x2 t = *reinterpret_cast<const f32x2*>(red + w * 32 + 2 * tid); s0 += t[0]; s1 += t[1]; }
    st_gran(ws + WS_QG + ((size_t)(tid >> 3) * NH + head) * 64 + 8 * g8 + (tid & 7), tag, f2bf(s0) | (f2bf(s1) << 16));
  }
  STAMP(12);
  SB();
  ISSUE(Bf, 1);
  SB();
  u64* const part_p = ws + WS_PART + ((size_t)b * 128 + head * 4 + split) * 512 + wave * 64 + c;
  u64* const ml_p = ws + WS_ML + (size_t)bh * 64 + (split * NW + wave) * 2;
  if (lane < 16) {
#pragma unroll
    for (int nt = 0; nt < 4; ++nt) st_gran(part_p + 16 * nt, epoch, 0u);
  }
  if (tid < 64) lds[L_OI + 128 + tid] = 0.f;
  if (lane < 2) st_gran(ml_p + lane, epoch, 0u);
  if (role == 1 && tid < 64) st_gran(ws + WS_HG + (tid >> 3) * 512 + rcol0 + (tid & 7), epoch, 0u);
  if (role == 2 && tid < 64) { st_gran(ws + WS_AG + (tid >> 4) * FF + rcol0 + c, epoch, 0u); st_gran(ws + WS_AG + ((tid >> 4) + 4) * FF + rcol0 + c, epoch, 0u); }
  u32 qp[4];
  {
    const u64* q0 = ws + WS_QG + (size_t)bh * 64 + 4 * c;
    const u64* pp[4] = {q0, q0 + 1, q0 + 2, q0 + 3};
    poll_gran<4>(pp, tag, qp);
  }

  STAMP(1);
  float mx = -INFINITY;
#define COMPUTE_K(BUF, j_) do { float x_[8]; \
    _Pragma("unroll") for (int t_ = 0; t_ < 8; ++t_) { float d_ = dot2(BUF[t_][0], qp[0], 0.f); d_ = dot2(BUF[t_][1], qp[1], d_); \
      d_ = dot2(BUF[t_][2], qp[2], d_); x_[t_] = dot2(BUF[t_][3], qp[3], d_); } \
    _Pragma("unroll") for (int h_ = 0; h_ < 4; ++h_) x_[h_] = redd<ROR8>(x_[h_], x_[h_ + 4], c & 8); \
    _Pragma("unroll") for (int h_ = 0; h_ < 2; ++h_) x_[h_] = redd<XOR7>(x_[h_], x_[h_ + 2], c & 4); \
    x_[0] = redd<XOR2>(x_[0], x_[1], c & 2); \
    x_[0] += dpp<XOR1>(x_[0]); \
    float s_ = rbf(rbf(x_[0]) * SCALE) * LOG2E; \
    mx = fmaxf(mx, s_); \
    sc[(j_) * 32 + r * 8 + (c >> 1)] = s_; } while (0)

  SB();
  COMPUTE_K(A, 0); SB(); ISSUE(A, 2); SB();
  COMPUTE_K(Bf, 1); SB(); ISSUE(Bf, 3); SB();
  u32x4 RW[4];
  {
    const unsigned wdst = __builtin_amdgcn_readfirstlane((unsigned)(size_t)((LAS float*)lds + L_WO) + (unsigned)wave * 16384u);
    const u16* p = wo + (size_t)(head * DH + 16 * wave) * DM;
#pragma unroll
    for (int i = 0; i < 16; ++i) {
      const int sw = 2 * ((i & 3) | (((i >> 3) & 1) << 2));
      glds16(p + (size_t)i * DM + 8 * (lane ^ sw), wdst + i * 1024);
    }
  }
  {
    const u16* p; size_t st;
    if (role == 0) {
      p = wd + (size_t)tid * DM + rcol0; st = (size_t)512 * DM;
    } else {
      p = wd; st = 0;
    }
#pragma unroll
    for (int i = 0; i < 3; ++i) RW[i] = *reinterpret_cast<const u32x4*>(p + (size_t)i * st);
    RW[3] = *reinterpret_cast<const u32x4*>(p + ((role == 0 && tid >= 256) ? (size_t)0 : 3 * st));
  }
  u16 GW[2][2][8];
  {
    const size_t goff = role == 2 ? (size_t)(64 * wave + 8 * r) * FF + rcol0 + c : (size_t)0;
    const size_t gst = role == 2 ? (size_t)FF : (size_t)0;
#pragma unroll
    for (int ks = 0; ks < 2; ++ks)
#pragma unroll
      for (int j = 0; j < 8; ++j) { GW[0][ks][j] = wg[goff + (32 * ks + j) * gst]; GW[1][ks][j] = wu[goff + (32 * ks + j) * gst]; }
  }
  SB();
#pragma unroll 1
  for (int j = 2; j < NT; j += 2) {
    COMPUTE_K(A, j); SB(); ISSUE(A, j + 2); SB();
    __builtin_amdgcn_s_barrier(); SB();
    COMPUTE_K(Bf, j + 1); SB(); ISSUE(Bf, j + 3); SB();
    __builtin_amdgcn_s_barrier(); SB();
  }
  STAMP(2);
  const float mw = wave_max(mx);
  {
    f32x4 s0 = *reinterpret_cast<const f32x4*>(sc + 8 * lane), s1 = *reinterpret_cast<const f32x4*>(sc + 8 * lane + 4);
    float l = 0.f;
#pragma unroll
    for (int k = 0; k < 4; ++k) { s0[k] = __builtin_amdgcn_exp2f(s0[k] - mw); s1[k] = __builtin_amdgcn_exp2f(s1[k] - mw); l += s0[k] + s1[k]; }
    *reinterpret_cast<f32x4*>(sc + 8 * lane) = s0;
    *reinterpret_cast<f32x4*>(sc + 8 * lane + 4) = s1;
    const float lw = wave_sum(l);
    if (lane < 2) st_gran(ml_p + lane, tag, __float_as_uint(lane == 0 ? mw : lw));
  }
  STAMP(3);

#define COMPUTE_V(BUF, j_) do { \
    f32x4 p0_ = *reinterpret_cast<const f32x4*>(sc + (j_) * 32 + r * 8), p1_ = *reinterpret_cast<const f32x4*>(sc + (j_) * 32 + r * 8 + 4); \
    _Pragma("unroll") for (int t_ = 0; t_ < 8; ++t_) { float p_ = t_ < 4 ? p0_[t_ & 3] : p1_[t_ & 3]; \
      asm volatile("" : "+v"(BUF[t_]));    \
      const f32x2 pp_ = {p_, p_}; \
      _Pragma("unroll") for (int k_ = 0; k_ < 4; ++k_) { \
        const f32x2 v_ = {lo2f(BUF[t_][k_]), hi2f(BUF[t_][k_])}; \
        acc2[k_] = __builtin_elementwise_fma(pp_, v_, acc2[k_]); } \
      if (t_ & 1) SB(); } } while (0)
  f32x2 acc2[4];
#pragma unroll
  for (int k = 0; k < 4; ++k) acc2[k] = f32x2{0.f, 0.f};
#pragma unroll 1
  for (int j = 0; j < NT - 2; j += 2) {
    COMPUTE_V(A, j); SB(); ISSUE(A, NT + j + 2); SB();
    __builtin_amdgcn_s_barrier(); SB();
    COMPUTE_V(Bf, j + 1); SB(); ISSUE(Bf, NT + j + 3); SB();
    __builtin_amdgcn_s_barrier(); SB();
  }
  bf16x8 WB[4][4];
  {
    const LAS char* wbase = (const LAS char*)((LAS float*)lds + L_WO);
    const int q_ = c >> 2, p_ = c & 3;
#pragma unroll
    for (int ks = 0; ks < 4; ++ks)
#pragma unroll
      for (int nt = 0; nt < 4; ++nt) {
        const int sw = 2 * (q_ | ((r & 1) << 2));
        const int ch = (2 * (4 * wave + nt) + (p_ >> 1)) ^ sw;
        const int row = 32 * ks + 8 * r + q_;
        const bf16x4 lo = __builtin_amdgcn_ds_read_tr16_b64_v4bf16((LAS bf16x4*)(wbase + row * 1024 + ch * 16 + 8 * (p_ & 1)));
        const bf16x4 hi = __builtin_amdgcn_ds_read_tr16_b64_v4bf16((LAS bf16x4*)(wbase + (row + 4) * 1024 + ch * 16 + 8 * (p_ & 1)));
        WB[ks][nt] = __builtin_shufflevector(lo, hi, 0, 1, 2, 3, 4, 5, 6, 7);
      }
  }
  float fnorm;
  {
    const u64* pp[1] = {ws + WS_ML + (size_t)bh * 64 + lane};
    u32 v[1];
    poll_gran<1>(pp, tag, v);
    float val = __uint_as_float(v[0]);
    float other = dpp<XOR1>(val);
    float mj = (lane & 1) ? other : val, lj = (lane & 1) ? val : other;
    float M = wave_max(mj);
    float L = wave_sum((lane & 1) ? 0.f : __builtin_amdgcn_exp2f(mj - M) * lj);
    fnorm = __builtin_amdgcn_exp2f(mw - M) / L;
  }
  SB();
  COMPUTE_V(A, NT - 2);
  COMPUTE_V(Bf, NT - 1);
  STAMP(4);
  {
    float t0 = red16(acc2[0][0], acc2[1][0]), t1 = red16(acc2[0][1], acc2[1][1]);
    float t2 = red16(acc2[2][0], acc2[3][0]), t3 = red16(acc2[2][1], acc2[3][1]);
    float u0 = red32(t0, t2), u1 = red32(t1, t3);
    *reinterpret_cast<f32x2*>(lds + L_QP + wave * 128 + 8 * c + 2 * r) = f32x2{u0 * fnorm, u1 * fnorm};
  }
  __syncthreads();
  STAMP(5);
  if (tid < 128) {
    float o = 0.f;
#pragma unroll
    for (int w = 0; w < NW; ++w) o += lds[L_QP + w * 128 + tid];
    const u32 hi = f2bf(o), lo = f2bf(o - bf2f(hi));
    u16* oi = reinterpret_cast<u16*>(lds + L_OI);
    oi[tid] = (u16)hi; oi[128 + tid] = (u16)lo;
  }
  __syncthreads();
  {
    const u16* oi = reinterpret_cast<const u16*>(lds + L_OI) + (c < 2 ? c : 2) * 128 + 8 * r;
    f32x4 D[4];
#pragma unroll
    for (int nt = 0; nt < 4; ++nt) D[nt] = f32x4{0.f, 0.f, 0.f, 0.f};
#pragma unroll
    for (int ks = 0; ks < 4; ++ks) {
      const bf16x8 af = *reinterpret_cast<const bf16x8*>(oi + 32 * ks);
#pragma unroll
      for (int nt = 0; nt < 4; ++nt) D[nt] = __builtin_amdgcn_mfma_f32_16x16x32_bf16(af, WB[ks][nt], D[nt], 0, 0, 0);
    }
    if (lane < 16) {
#pragma unroll
      for (int nt = 0; nt < 4; ++nt) st_gran(part_p + 16 * nt, tag, __float_as_uint(D[nt][0] + D[nt][1]));
    }
  }
  STAMP(6);

  if (role == 1) {
    const int seg = tid >> 1, half = tid & 1;
    const u64* p0 = ws + WS_PART + (size_t)seg * 512 + rcol0 + 4 * half;
    const u64* pp[4] = {p0, p0 + 1, p0 + 2, p0 + 3};
    const float xr0 = bf2f(x[rcol0 + (tid & 7)]), xr1 = bf2f(x[DM + rcol0 + (tid & 7)]);
    u32 v[4];
    poll_gran<4>(pp, tag, v);
    STAMP(7);
    float f0 = red32(__uint_as_float(v[0]), __uint_as_float(v[2])), f1 = red32(__uint_as_float(v[1]), __uint_as_float(v[3]));
    f0 = red16(f0, f1);
    f0 += dpp<ROR8>(f0); f0 += dpp<ROR4>(f0); f0 += dpp<XOR2>(f0);
    if ((lane & 14) == 0) red[wave * 8 + 4 * half + (lane >> 4)] = f0;
    __syncthreads();
    if (tid < 64) {
      const int e = tid & 7;
      float s0 = (red[e] + red[8 + e]) + (red[16 + e] + red[24 + e]);
      float s1 = (red[32 + e] + red[40 + e]) + (red[48 + e] + red[56 + e]);
      u32 h0 = f2bf(xr0 + rbf(s0));
      u32 h1 = f2bf(xr1 + rbf(s1));
      st_gran(ws + WS_HG + (tid >> 3) * 512 + rcol0 + e, tag, h0 | (h1 << 16));
    }
    STAMP(8);
  } else if (role == 2) {
    float* const p_st = lds + L_QP;
    u16* const p_ln = reinterpret_cast<u16*>(lds + L_SC);
    bf16x8 GB[2][2];
#pragma unroll
    for (int mt = 0; mt < 2; ++mt)
#pragma unroll
      for (int ks = 0; ks < 2; ++ks) {
        u32x4 t;
#pragma unroll
        for (int j = 0; j < 4; ++j) t[j] = (u32)GW[mt][ks][2 * j] | ((u32)GW[mt][ks][2 * j + 1] << 16);
        GB[mt][ks] = __builtin_bit_cast(bf16x8, t);
      }
    p_ln[1024 + tid] = 0;
    float h0, h1;
    {
      const u64* pp[1] = {ws + WS_HG + xcd * 512 + tid};
      u32 v[1];
      poll_gran<1>(pp, tag, v);
      STAMP(7);
      h0 = lo2f(v[0]); h1 = hi2f(v[0]);
      const float mu0 = wave_sum(h0) * (1.f / 64.f), mu1 = wave_sum(h1) * (1.f / 64.f);
      const float q0 = wave_sum((h0 - mu0) * (h0 - mu0)), q1 = wave_sum((h1 - mu1) * (h1 - mu1));
      if (lane == 0) *reinterpret_cast<f32x4*>(p_st + 4 * wave) = f32x4{mu0, q0, mu1, q1};
    }
    __syncthreads();
    {
      f32x4 st[8];
#pragma unroll
      for (int w = 0; w < NW; ++w) st[w] = *reinterpret_cast<const f32x4*>(p_st + 4 * w);
      float m0 = 0.f, m1 = 0.f, v0 = 0.f, v1 = 0.f;
#pragma unroll
      for (int w = 0; w < NW; ++w) { m0 += st[w][0]; m1 += st[w][2]; v0 += st[w][1]; v1 += st[w][3]; }
      m0 *= 0.125f; m1 *= 0.125f;
#pragma unroll
      for (int w = 0; w < NW; ++w) { v0 = fmaf(64.f * (st[w][0] - m0), st[w][0] - m0, v0); v1 = fmaf(64.f * (st[w][2] - m1), st[w][2] - m1, v1); }
      const float d0 = rbf(sqrtf(rbf(rbf(v0 * (1.f / 512.f)) + 1e-5f))), d1 = rbf(sqrtf(rbf(rbf(v1 * (1.f / 512.f)) + 1e-5f)));
      p_ln[tid] = (u16)f2bf(rbf(h0 - rbf(m0)) / d0);
      p_ln[512 + tid] = (u16)f2bf(rbf(h1 - rbf(m1)) / d1);
    }
    __syncthreads();
    {
      const u16* ap = p_ln + (c < 2 ? c : 2) * 512 + 64 * wave + 8 * r;
      const bf16x8 a0 = *reinterpret_cast<const bf16x8*>(ap), a1 = *reinterpret_cast<const bf16x8*>(ap + 32);
      f32x4 Dg = {0.f, 0.f, 0.f, 0.f}, Du = {0.f, 0.f, 0.f, 0.f};
      Dg = __builtin_amdgcn_mfma_f32_16x16x32_bf16(a0, GB[0][0], Dg, 0, 0, 0);
      Du = __builtin_amdgcn_mfma_f32_16x16x32_bf16(a0, GB[1][0], Du, 0, 0, 0);
      Dg = __builtin_amdgcn_mfma_f32_16x16x32_bf16(a1, GB[0][1], Dg, 0, 0, 0);
      Du = __builtin_amdgcn_mfma_f32_16x16x32_bf16(a1, GB[1][1], Du, 0, 0, 0);
      if (lane < 16) *reinterpret_cast<f32x4*>(lds + L_QP + 64 + (wave * 16 + lane) * 4) = f32x4{Dg[0], Dg[1], Du[0], Du[1]};
    }
    __syncthreads();
    if (tid < 64) {
      float g = 0.f, u = 0.f;
#pragma unroll
      for (int w = 0; w < NW; ++w) { g += lds[L_QP + 64 + (w * 16 + c) * 4 + (r & 1)]; u += lds[L_QP + 64 + (w * 16 + c) * 4 + 2 + (r & 1)]; }
      g = rbf(g); u = rbf(u);
      const float sg = rbf(g / (1.f + __expf(-g)));
      const u32 av = f2bf(sg * u);
      auto t = __builtin_amdgcn_permlane16_swap(av, av, false, false);
      const u32 pk = t[0] | (t[1] << 16);
      st_gran(ws + WS_AG + r * FF + rcol0 + c, tag, pk);
      st_gran(ws + WS_AG + (r + 4) * FF + rcol0 + c, tag, pk);
    }
    STAMP(8);
  } else if (role == 0) {
    const u64* ap = ws + WS_AG + xcd * FF + tid;
    const u64* pp[5] = {ap, ap + 512, ap + 1024, tid < 256 ? ap + 1536 : ap, ws + WS_HG + xcd * 512 + rcol0 + (tid & 7)};
    u32 v[5];
    poll_gran<5>(pp, tag, v);
    STAMP(7);
    f32x2 a0[4], a1[4];
#pragma unroll
    for (int k = 0; k < 4; ++k) { a0[k] = f32x2{0.f, 0.f}; a1[k] = f32x2{0.f, 0.f}; }
#pragma unroll
    for (int i = 0; i < 4; ++i) {
      float l0 = lo2f(v[i]), l1 = hi2f(v[i]);
      if (i == 3 && tid >= 256) { l0 = 0.f; l1 = 0.f; }
      fma8(a0, l0, RW[i]);
      fma8(a1, l1, RW[i]);
    }
    float y[8];
#pragma unroll
    for (int k = 0; k < 4; ++k) { y[2 * k] = red32(a0[k][0], a1[k][0]); y[2 * k + 1] = red32(a0[k][1], a1[k][1]); }
#pragma unroll
    for (int k = 0; k < 4; ++k) y[k] = red16(y[k], y[k + 4]);
#pragma unroll
    for (int k = 0; k < 2; ++k) y[k] = redd<ROR8>(y[k], y[k + 2], c & 8);
    y[0] = redd<XOR7>(y[0], y[1], c & 4);
    y[0] += dpp<XOR2>(y[0]);
    y[0] += dpp<XOR1>(y[0]);
    if ((lane & 3) == 0) red[wave * 16 + (lane >> 2)] = y[0];
    __syncthreads();
    if (tid < 16) {
      float s = 0.f;
#pragma unroll
      for (int w = 0; w < NW; ++w) s += red[w * 16 + tid];
      float hh = (tid < 8) ? lo2f(v[4]) : hi2f(v[4]);
      out[(tid >> 3) * DM + rcol0 + (tid & 7)] = (u16)f2bf(hh + rbf(s));
    }
    STAMP(8);
    if (bid == 0 && tid == 0) __hip_atomic_store((u32*)(ws + WS_E), tag, __ATOMIC_RELAXED, __HIP_MEMORY_SCOPE_AGENT);
  }
}

extern "C" void kernel_launch(void* const* d_in, const int* in_sizes, int n_in,
                              void* d_out, int out_size, void* d_ws, size_t ws_size,
                              hipStream_t stream) {
  fused_kernel<<<256, NTHR, 0, stream>>>((const u16*)d_in[0], (const u16*)d_in[1], (const u16*)d_in[2], (const u16*)d_in[3],
                                         (const u16*)d_in[4], (const u16*)d_in[6], (const u16*)d_in[7], (const u16*)d_in[8],
                                         (u16*)d_out, (u64*)d_ws);
}
```

```cpp
#include <hip/hip_runtime.h>
#include <hip/hip_bf16.h>
#include <stdint.h>

typedef unsigned int u32;
typedef unsigned short u16;
typedef unsigned long long u64;
typedef __attribute__((ext_vector_type(4))) unsigned u32x4;
typedef __attribute__((ext_vector_type(4))) float f32x4;
typedef __attribute__((ext_vector_type(2))) float f32x2;
typedef __attribute__((ext_vector_type(2))) __bf16 bf16x2;
typedef __attribute__((ext_vector_type(4))) __bf16 bf16x4;
typedef __attribute__((ext_vector_type(8))) __bf16 bf16x8;

constexpr int NB = 2, DM = 512, NH = 32, SK = 16384, DH = 128, FF = 1792;
constexpr int NSPLIT = 4;
constexpr int NW = 8;
constexpr int NTHR = NW * 64;
constexpr int KPW = SK / NSPLIT / NW;
constexpr int TK = 32;
constexpr int NT = KPW / TK;
constexpr float SCALE = 0.08838834764831845f;
constexpr float LOG2E = 1.4426950408889634f;
constexpr unsigned SPIN_CAP = 1u << 22;

constexpr size_t WS_E = 0;
constexpr size_t WS_QG = 64;
constexpr size_t WS_ML = WS_QG + 2 * 64 * 64;
constexpr size_t WS_PART = WS_ML + 64 * 64;
constexpr size_t WS_HG = WS_PART + 2 * 128 * 512;
constexpr size_t WS_AG = WS_HG + 8 * 512;

__device__ __forceinline__ float bf2f(u32 v) { return __uint_as_float(v << 16); }
__device__ __forceinline__ float lo2f(u32 v) { return __uint_as_float(v << 16); }
__device__ __forceinline__ float hi2f(u32 v) { return __uint_as_float(v & 0xffff0000u); }
__device__ __forceinline__ float rbf(float x) { return (float)(__bf16)x; }
__device__ __forceinline__ u32 f2bf(float x) { __bf16 b = (__bf16)x; return (u32)__builtin_bit_cast(u16, b); }
__device__ __forceinline__ float dot2(u32 a, u32 b, float acc) {
  return __builtin_amdgcn_fdot2_f32_bf16(__builtin_bit_cast(bf16x2, a), __builtin_bit_cast(bf16x2, b), acc, false);
}
template <int CTRL> __device__ __forceinline__ float dpp(float x) {
  return __builtin_bit_cast(float, __builtin_amdgcn_mov_dpp(__builtin_bit_cast(int, x), CTRL, 0xf, 0xf, true));
}
constexpr int XOR1 = 0xB1, XOR2 = 0x4E, XOR7 = 0x141, ROR8 = 0x128, ROR4 = 0x124, ROR2 = 0x122, ROR1 = 0x121;
__device__ __forceinline__ float red32(float a, float b) {
  auto t = __builtin_amdgcn_permlane32_swap(__float_as_uint(a), __float_as_uint(b), false, false);
  return __uint_as_float(t[0]) + __uint_as_float(t[1]);
}
__device__ __forceinline__ float red16(float a, float b) {
  auto t = __builtin_amdgcn_permlane16_swap(__float_as_uint(a), __float_as_uint(b), false, false);
  return __uint_as_float(t[0]) + __uint_as_float(t[1]);
}
template <int CTRL> __device__ __forceinline__ float redd(float a, float b, bool hi) {
  const float t1 = a + dpp<CTRL>(a), t2 = b + dpp<CTRL>(b);
  return hi ? t2 : t1;
}
__device__ __forceinline__ float xrow_sum(float x) { x = red16(x, x); return red32(x, x); }
__device__ __forceinline__ float xrow_max(float x) {
  auto s = __builtin_amdgcn_permlane16_swap(__float_as_uint(x), __float_as_uint(x), false, false);
  x = fmaxf(__uint_as_float(s[0]), __uint_as_float(s[1]));
  auto t = __builtin_amdgcn_permlane32_swap(__float_as_uint(x), __float_as_uint(x), false, false);
  return fmaxf(__uint_as_float(t[0]), __uint_as_float(t[1]));
}
__device__ __forceinline__ float wave_sum(float x) {
  x += dpp<ROR8>(x); x += dpp<ROR4>(x); x += dpp<ROR2>(x); x += dpp<ROR1>(x);
  return xrow_sum(x);
}
__device__ __forceinline__ float wave_max(float x) {
  x = fmaxf(x, dpp<ROR8>(x)); x = fmaxf(x, dpp<ROR4>(x)); x = fmaxf(x, dpp<ROR2>(x)); x = fmaxf(x, dpp<ROR1>(x));
  return xrow_max(x);
}
__device__ __forceinline__ void st_gran(u64* p, u32 tag, u32 val) {
  __hip_atomic_store(p, ((u64)tag << 32) | (u64)val, __ATOMIC_RELAXED, __HIP_MEMORY_SCOPE_AGENT);
}
__device__ __forceinline__ u64 ld_gran(const u64* p) {
  return __hip_atomic_load(p, __ATOMIC_RELAXED, __HIP_MEMORY_SCOPE_AGENT);
}
template <int N> __device__ __forceinline__ void poll_gran(const u64* const (&p)[N], u32 tag, u32 (&v)[N]) {
  for (unsigned spins = 0;; ++spins) {
    bool ok = true;
#pragma unroll
    for (int k = 0; k < N; ++k) { u64 g = ld_gran(p[k]); v[k] = (u32)g; ok &= ((u32)(g >> 32) == tag); }
    if (__all(ok) || spins > SPIN_CAP) break;
    __builtin_amdgcn_s_sleep(1);
  }
}
__device__ __forceinline__ void fma8(f32x2 (&a)[4], float s, u32x4 w) {
  const f32x2 ss = {s, s};
#pragma unroll
  for (int k = 0; k < 4; ++k) { const f32x2 v = {lo2f(w[k]), hi2f(w[k])}; a[k] = __builtin_elementwise_fma(ss, v, a[k]); }
  asm volatile("" : "+v"(a[0]), "+v"(a[1]), "+v"(a[2]), "+v"(a[3]));
}
__device__ __forceinline__ void wave_ln_stats2(const float (&x)[8], const float (&y)[8], float& m0, float& d0, float& m1, float& d1) {
  float s0 = ((x[0] + x[1]) + (x[2] + x[3])) + ((x[4] + x[5]) + (x[6] + x[7]));
  float s1 = ((y[0] + y[1]) + (y[2] + y[3])) + ((y[4] + y[5]) + (y[6] + y[7]));
  s0 = wave_sum(s0) * (1.f / 512.f); s1 = wave_sum(s1) * (1.f / 512.f);
  float v0 = 0.f, v1 = 0.f;
#pragma unroll
  for (int k = 0; k < 8; ++k) { v0 += (x[k] - s0) * (x[k] - s0); v1 += (y[k] - s1) * (y[k] - s1); }
  v0 = wave_sum(v0) * (1.f / 512.f); v1 = wave_sum(v1) * (1.f / 512.f);
  m0 = rbf(s0); m1 = rbf(s1);
  d0 = rbf(sqrtf(rbf(rbf(v0) + 1e-5f))); d1 = rbf(sqrtf(rbf(rbf(v1) + 1e-5f)));
}

#define SB() __builtin_amdgcn_sched_barrier(0)
#define LAS __attribute__((address_space(3)))
#define FENCE() do { asm volatile("" ::: "memory"); __builtin_amdgcn_sched_barrier(0); } while (0)
__device__ __forceinline__ void glds16(const void* gsrc, unsigned lds_dst) {
  unsigned keep;
  asm volatile("s_mov_b32 %0, m0\n\ts_mov_b32 m0, %2\n\ts_nop 0\n\tglobal_load_lds_dwordx4 %1, off\n\ts_mov_b32 m0, %0" : "=&s"(keep) : "v"(gsrc), "s"(lds_dst) : "memory");
}
constexpr int L_SC = 0;
constexpr int L_QP = L_SC + NW * 512;
constexpr int L_RED = L_QP + NW * 128;
constexpr int L_OI = L_RED + 256;
constexpr int L_WO = L_OI + 192;
constexpr int L_TOT = L_WO + NW * 16 * 256;

__global__ void __launch_bounds__(NTHR, 2) fused_kernel(const u16* __restrict__ x, const u16* __restrict__ kc, const u16* __restrict__ vc,
    const u16* __restrict__ wq, const u16* __restrict__ wo, const u16* __restrict__ wg, const u16* __restrict__ wu,
    const u16* __restrict__ wd, u16* __restrict__ out, u64* __restrict__ ws) {
  __shared__ __attribute__((aligned(16))) float lds[L_TOT];
  const int tid = threadIdx.x, lane = tid & 63, r = lane >> 4, c = lane & 15;
  const int wave = __builtin_amdgcn_readfirstlane(tid >> 6);
  const int bid = blockIdx.x, xcd = bid & 7, idx = bid >> 3;
  const int head = xcd * 4 + (idx & 3), g8 = idx >> 2, b = g8 & 1, split = g8 >> 1;
  const int bh = b * NH + head;
  const int role = idx < 8 ? 0 : (idx < 16 ? 1 : (idx < 30 ? 2 : 3));
  const int rcol0 = role == 0 ? 8 * (xcd * 8 + idx) : (role == 2 ? 16 * (xcd * 14 + idx - 16) : 8 * (xcd * 8 + ((idx - 8) & 7)));
  float* const sc = lds + L_SC + wave * 512;
  float* const red = lds + L_RED;

  const u32 epoch = (u32)__hip_atomic_load((const u32*)(ws + WS_E), __ATOMIC_RELAXED, __HIP_MEMORY_SCOPE_AGENT);
  const u32x4 xa = *reinterpret_cast<const u32x4*>(x + 8 * lane), xb = *reinterpret_cast<const u32x4*>(x + DM + 8 * lane);
  const u32 xo0 = x[tid], xo1 = x[DM + tid];
  const u32x4 wq0 = *reinterpret_cast<const u32x4*>(wq + (size_t)tid * 4096 + head * DH + 16 * g8);
  const u32x4 wq1 = *reinterpret_cast<const u32x4*>(wq + (size_t)tid * 4096 + head * DH + 16 * g8 + 8);
  const size_t base = ((size_t)bh * SK + (size_t)split * (SK / NSPLIT) + (size_t)wave * TK) * DH + (size_t)lane * 8;
  const u16* kb = kc + base;
  const u16* vb = vc + base;
  const int rot = head & (NT - 1);
  u32x4 A[8], Bf[8];
#define ISSUE(BUF, j_) do { const u16* s_ = ((j_) < NT ? kb : vb) + (size_t)(((j_) + rot) & (NT - 1)) * (NW * TK * DH); \
    _Pragma("unroll") for (int t_ = 0; t_ < 8; ++t_) BUF[t_] = __builtin_nontemporal_load(reinterpret_cast<const u32x4*>(s_ + t_ * 4 * DH)); } while (0)
  SB();
  __builtin_amdgcn_s_barrier();
  SB();
  ISSUE(A, 0);
  SB();

  const u32 tag = epoch + 1u;
  {
    float xf[8], yf[8], m0, d0, m1, d1;
#pragma unroll
    for (int k = 0; k < 4; ++k) { xf[2 * k] = lo2f(xa[k]); xf[2 * k + 1] = hi2f(xa[k]); yf[2 * k] = lo2f(xb[k]); yf[2 * k + 1] = hi2f(xb[k]); }
    wave_ln_stats2(xf, yf, m0, d0, m1, d1);
    const float l0 = rbf(rbf(bf2f(xo0) - m0) / d0), l1 = rbf(rbf(bf2f(xo1) - m1) / d1);
    float y[32];
#pragma unroll
    for (int k = 0; k < 4; ++k) {
      y[2 * k] = l0 * lo2f(wq0[k]); y[2 * k + 1] = l0 * hi2f(wq0[k]); y[8 + 2 * k] = l0 * lo2f(wq1[k]); y[8 + 2 * k + 1] = l0 * hi2f(wq1[k]);
      y[16 + 2 * k] = l1 * lo2f(wq0[k]); y[16 + 2 * k + 1] = l1 * hi2f(wq0[k]); y[24 + 2 * k] = l1 * lo2f(wq1[k]); y[24 + 2 * k + 1] = l1 * hi2f(wq1[k]);
    }
#pragma unroll
    for (int i = 0; i < 16; ++i) y[i] = red32(y[i], y[i + 16]);
#pragma unroll
    for (int i = 0; i < 8; ++i) y[i] = red16(y[i], y[i + 8]);
#pragma unroll
    for (int i = 0; i < 4; ++i) y[i] = redd<ROR8>(y[i], y[i + 4], c & 8);
#pragma unroll
    for (int i = 0; i < 2; ++i) y[i] = redd<XOR7>(y[i], y[i + 2], c & 4);
    y[0] = redd<XOR2>(y[0], y[1], c & 2);
    y[0] += dpp<XOR1>(y[0]);
    red[wave * 32 + (lane >> 1)] = y[0];
  }
  __syncthreads();
  if (tid < 16) {
    float s0 = 0.f, s1 = 0.f;
#pragma unroll
    for (int w = 0; w < NW; ++w) { f32x2 t = *reinterpret_cast<const f32x2*>(red + w * 32 + 2 * tid); s0 += t[0]; s1 += t[1]; }
    u64* qg = ws + WS_QG + ((size_t)(tid >> 3) * NH + head) * 64 + 8 * g8 + (tid & 7);
    const u32 qv = f2bf(s0) | (f2bf(s1) << 16);
    st_gran(qg, tag, qv);
    st_gran(qg + 64 * 64, ~tag, qv);
  }
  SB();
  ISSUE(Bf, 1);
  SB();
  u64* const part_p = ws + WS_PART + ((size_t)b * 128 + head * 4 + split) * 512 + wave * 64 + c;
  u64* const ml_p = ws + WS_ML + (size_t)bh * 64 + (split * NW + wave) * 2;
  if (lane < 16) {
#pragma unroll
    for (int nt = 0; nt < 4; ++nt) st_gran(part_p + 16 * nt, epoch, 0u);
  }
  if (tid < 64) lds[L_OI + 128 + tid] = 0.f;
  if (lane < 2) st_gran(ml_p + lane, epoch, 0u);
  if (role == 1 && tid < 64) st_gran(ws + WS_HG + (tid >> 3) * 512 + rcol0 + (tid & 7), epoch, 0u);
  if (role == 2 && tid < 64) { st_gran(ws + WS_AG + (tid >> 4) * FF + rcol0 + c, epoch, 0u); st_gran(ws + WS_AG + ((tid >> 4) + 4) * FF + rcol0 + c, epoch, 0u); }
  u32 qp[4];
  {
    const u64* q0 = ws + WS_QG + (size_t)bh * 64 + 4 * c;
    for (unsigned spins = 0;; ++spins) {
      bool ok = true;
#pragma unroll
      for (int k = 0; k < 4; ++k) {
        const u64 g0 = ld_gran(q0 + k), g1 = ld_gran(q0 + 64 * 64 + k);
        qp[k] = (u32)g0;
        ok &= ((u32)(g0 >> 32) == tag) && ((u32)(g1 >> 32) == ~tag) && ((u32)g0 == (u32)g1);
      }
      if (__all(ok) || spins > SPIN_CAP) break;
      __builtin_amdgcn_s_sleep(1);
    }
  }

  float mx = -INFINITY;
#define COMPUTE_K(BUF, j_) do { float x_[8]; \
    _Pragma("unroll") for (int t_ = 0; t_ < 8; ++t_) { float d_ = dot2(BUF[t_][0], qp[0], 0.f); d_ = dot2(BUF[t_][1], qp[1], d_); \
      d_ = dot2(BUF[t_][2], qp[2], d_); x_[t_] = dot2(BUF[t_][3], qp[3], d_); } \
    _Pragma("unroll") for (int h_ = 0; h_ < 4; ++h_) x_[h_] = redd<ROR8>(x_[h_], x_[h_ + 4], c & 8); \
    _Pragma("unroll") for (int h_ = 0; h_ < 2; ++h_) x_[h_] = redd<XOR7>(x_[h_], x_[h_ + 2], c & 4); \
    x_[0] = redd<XOR2>(x_[0], x_[1], c & 2); \
    x_[0] += dpp<XOR1>(x_[0]); \
    float s_ = rbf(rbf(x_[0]) * SCALE) * LOG2E; \
    mx = fmaxf(mx, s_); \
    sc[(j_) * 32 + r * 8 + (c >> 1)] = s_; } while (0)

  SB();
  COMPUTE_K(A, 0); SB(); ISSUE(A, 2); SB();
  COMPUTE_K(Bf, 1); SB(); ISSUE(Bf, 3); SB();
  u32x4 RW[4];
  {
    const unsigned wdst = __builtin_amdgcn_readfirstlane((unsigned)(size_t)((LAS float*)lds + L_WO) + (unsigned)wave * 16384u);
    const u16* p = wo + (size_t)(head * DH + 16 * wave) * DM;
#pragma unroll
    for (int i = 0; i < 16; ++i) {
      const int sw = 2 * ((i & 3) | (((i >> 3) & 1) << 2));
      glds16(p + (size_t)i * DM + 8 * (lane ^ sw), wdst + i * 1024);
    }
  }
  {
    const u16* p; size_t st;
    if (role == 0) {
      p = wd + (size_t)tid * DM + rcol0; st = (size_t)512 * DM;
    } else {
      p = wd; st = 0;
    }
#pragma unroll
    for (int i = 0; i < 3; ++i) RW[i] = *reinterpret_cast<const u32x4*>(p + (size_t)i * st);
    RW[3] = *reinterpret_cast<const u32x4*>(p + ((role == 0 && tid >= 256) ? (size_t)0 : 3 * st));
  }
  u16 GW[2][2][8];
  {
    const size_t goff = role == 2 ? (size_t)(64 * wave + 8 * r) * FF + rcol0 + c : (size_t)0;
    const size_t gst = role == 2 ? (size_t)FF : (size_t)0;
#pragma unroll
    for (int ks = 0; ks < 2; ++ks)
#pragma unroll
      for (int j = 0; j < 8; ++j) { GW[0][ks][j] = wg[goff + (32 * ks + j) * gst]; GW[1][ks][j] = wu[goff + (32 * ks + j) * gst]; }
  }
  SB();
#pragma unroll 1
  for (int j = 2; j < NT; j += 2) {
    COMPUTE_K(A, j); SB(); ISSUE(A, j + 2); SB();
    __builtin_amdgcn_s_barrier(); SB();
    COMPUTE_K(Bf, j + 1); SB(); ISSUE(Bf, j + 3); SB();
    __builtin_amdgcn_s_barrier(); SB();
  }
  const float mw = wave_max(mx);
  {
    f32x4 s0 = *reinterpret_cast<const f32x4*>(sc + 8 * lane), s1 = *reinterpret_cast<const f32x4*>(sc + 8 * lane + 4);
    float l = 0.f;
#pragma unroll
    for (int k = 0; k < 4; ++k) { s0[k] = __builtin_amdgcn_exp2f(s0[k] - mw); s1[k] = __builtin_amdgcn_exp2f(s1[k] - mw); l += s0[k] + s1[k]; }
    *reinterpret_cast<f32x4*>(sc + 8 * lane) = s0;
    *reinterpret_cast<f32x4*>(sc + 8 * lane + 4) = s1;
    const float lw = wave_sum(l);
    if (lane < 2) st_gran(ml_p + lane, tag, __float_as_uint(lane == 0 ? mw : lw));
  }

#define COMPUTE_V(BUF, j_) do { \
    f32x4 p0_ = *reinterpret_cast<const f32x4*>(sc + (j_) * 32 + r * 8), p1_ = *reinterpret_cast<const f32x4*>(sc + (j_) * 32 + r * 8 + 4); \
    _Pragma("unroll") for (int t_ = 0; t_ < 8; ++t_) { float p_ = t_ < 4 ? p0_[t_ & 3] : p1_[t_ & 3]; \
      asm volatile("" : "+v"(BUF[t_]));    \
      const f32x2 pp_ = {p_, p_}; \
      _Pragma("unroll") for (int k_ = 0; k_ < 4; ++k_) { \
        const f32x2 v_ = {lo2f(BUF[t_][k_]), hi2f(BUF[t_][k_])}; \
        acc2[k_] = __builtin_elementwise_fma(pp_, v_, acc2[k_]); } \
      if (t_ & 1) SB(); } } while (0)
  f32x2 acc2[4];
#pragma unroll
  for (int k = 0; k < 4; ++k) acc2[k] = f32x2{0.f, 0.f};
#pragma unroll 1
  for (int j = 0; j < NT - 2; j += 2) {
    COMPUTE_V(A, j); SB(); ISSUE(A, NT + j + 2); SB();
    __builtin_amdgcn_s_barrier(); SB();
    COMPUTE_V(Bf, j + 1); SB(); ISSUE(Bf, NT + j + 3); SB();
    __builtin_amdgcn_s_barrier(); SB();
  }
  bf16x8 WB[4][4];
  {
    const LAS char* wbase = (const LAS char*)((LAS float*)lds + L_WO);
    const int q_ = c >> 2, p_ = c & 3;
#pragma unroll
    for (int ks = 0; ks < 4; ++ks)
#pragma unroll
      for (int nt = 0; nt < 4; ++nt) {
        const int sw = 2 * (q_ | ((r & 1) << 2));
        const int ch = (2 * (4 * wave + nt) + (p_ >> 1)) ^ sw;
        const int row = 32 * ks + 8 * r + q_;
        const bf16x4 lo = __builtin_amdgcn_ds_read_tr16_b64_v4bf16((LAS bf16x4*)(wbase + row * 1024 + ch * 16 + 8 * (p_ & 1)));
        const bf16x4 hi = __builtin_amdgcn_ds_read_tr16_b64_v4bf16((LAS bf16x4*)(wbase + (row + 4) * 1024 + ch * 16 + 8 * (p_ & 1)));
        WB[ks][nt] = __builtin_shufflevector(lo, hi, 0, 1, 2, 3, 4, 5, 6, 7);
      }
  }
  float fnorm;
  {
    const u64* pp[1] = {ws + WS_ML + (size_t)bh * 64 + lane};
    u32 v[1];
    poll_gran<1>(pp, tag, v);
    float val = __uint_as_float(v[0]);
    float other = dpp<XOR1>(val);
    float mj = (lane & 1) ? other : val, lj = (lane & 1) ? val : other;
    float M = wave_max(mj);
    float L = wave_sum((lane & 1) ? 0.f : __builtin_amdgcn_exp2f(mj - M) * lj);
    fnorm = __builtin_amdgcn_exp2f(mw - M) / L;
  }
  SB();
  COMPUTE_V(A, NT - 2);
  COMPUTE_V(Bf, NT - 1);
  {
    float t0 = red16(acc2[0][0], acc2[1][0]), t1 = red16(acc2[0][1], acc2[1][1]);
    float t2 = red16(acc2[2][0], acc2[3][0]), t3 = red16(acc2[2][1], acc2[3][1]);
    float u0 = red32(t0, t2), u1 = red32(t1, t3);
    *reinterpret_cast<f32x2*>(lds + L_QP + wave * 128 + 8 * c + 2 * r) = f32x2{u0 * fnorm, u1 * fnorm};
  }
  __syncthreads();
  if (tid < 128) {
    float o = 0.f;
#pragma unroll
    for (int w = 0; w < NW; ++w) o += lds[L_QP + w * 128 + tid];
    const u32 hi = f2bf(o), lo = f2bf(o - bf2f(hi));
    u16* oi = reinterpret_cast<u16*>(lds + L_OI);
    oi[tid] = (u16)hi; oi[128 + tid] = (u16)lo;
  }
  __syncthreads();
  {
    const u16* oi = reinterpret_cast<const u16*>(lds + L_OI) + (c < 2 ? c : 2) * 128 + 8 * r;
    f32x4 D[4];
#pragma unroll
    for (int nt = 0; nt < 4; ++nt) D[nt] = f32x4{0.f, 0.f, 0.f, 0.f};
#pragma unroll
    for (int ks = 0; ks < 4; ++ks) {
      const bf16x8 af = *reinterpret_cast<const bf16x8*>(oi + 32 * ks);
#pragma unroll
      for (int nt = 0; nt < 4; ++nt) D[nt] = __builtin_amdgcn_mfma_f32_16x16x32_bf16(af, WB[ks][nt], D[nt], 0, 0, 0);
    }
    if (lane < 16) {
#pragma unroll
      for (int nt = 0; nt < 4; ++nt) st_gran(part_p + 16 * nt, tag, __float_as_uint(D[nt][0] + D[nt][1]));
    }
  }

  if (role == 1) {
    const int seg = tid >> 1, half = tid & 1;
    const u64* p0 = ws + WS_PART + (size_t)seg * 512 + rcol0 + 4 * half;
    const u64* pp[4] = {p0, p0 + 1, p0 + 2, p0 + 3};
    const float xr0 = bf2f(x[rcol0 + (tid & 7)]), xr1 = bf2f(x[DM + rcol0 + (tid & 7)]);
    u32 v[4];
    poll_gran<4>(pp, tag, v);
    float f0 = red32(__uint_as_float(v[0]), __uint_as_float(v[2])), f1 = red32(__uint_as_float(v[1]), __uint_as_float(v[3]));
    f0 = red16(f0, f1);
    f0 += dpp<ROR8>(f0); f0 += dpp<ROR4>(f0); f0 += dpp<XOR2>(f0);
    if ((lane & 14) == 0) red[wave * 8 + 4 * half + (lane >> 4)] = f0;
    __syncthreads();
    if (tid < 64) {
      const int e = tid & 7;
      float s0 = (red[e] + red[8 + e]) + (red[16 + e] + red[24 + e]);
      float s1 = (red[32 + e] + red[40 + e]) + (red[48 + e] + red[56 + e]);
      u32 h0 = f2bf(xr0 + rbf(s0));
      u32 h1 = f2bf(xr1 + rbf(s1));
      st_gran(ws + WS_HG + (tid >> 3) * 512 + rcol0 + e, tag, h0 | (h1 << 16));
    }
  } else if (role == 2) {
    float* const p_st = lds + L_QP;
    u16* const p_ln = reinterpret_cast<u16*>(lds + L_SC);
    bf16x8 GB[2][2];
#pragma unroll
    for (int mt = 0; mt < 2; ++mt)
#pragma unroll
      for (int ks = 0; ks < 2; ++ks) {
        u32x4 t;
#pragma unroll
        for (int j = 0; j < 4; ++j) t[j] = (u32)GW[mt][ks][2 * j] | ((u32)GW[mt][ks][2 * j + 1] << 16);
        GB[mt][ks] = __builtin_bit_cast(bf16x8, t);
      }
    p_ln[1024 + tid] = 0;
    float h0, h1;
    {
      const u64* pp[1] = {ws + WS_HG + xcd * 512 + tid};
      u32 v[1];
      poll_gran<1>(pp, tag, v);
      h0 = lo2f(v[0]); h1 = hi2f(v[0]);
      const float mu0 = wave_sum(h0) * (1.f / 64.f), mu1 = wave_sum(h1) * (1.f / 64.f);
      const float q0 = wave_sum((h0 - mu0) * (h0 - mu0)), q1 = wave_sum((h1 - mu1) * (h1 - mu1));
      if (lane == 0) *reinterpret_cast<f32x4*>(p_st + 4 * wave) = f32x4{mu0, q0, mu1, q1};
    }
    __syncthreads();
    {
      f32x4 st[8];
#pragma unroll
      for (int w = 0; w < NW; ++w) st[w] = *reinterpret_cast<const f32x4*>(p_st + 4 * w);
      float m0 = 0.f, m1 = 0.f, v0 = 0.f, v1 = 0.f;
#pragma unroll
      for (int w = 0; w < NW; ++w) { m0 += st[w][0]; m1 += st[w][2]; v0 += st[w][1]; v1 += st[w][3]; }
      m0 *= 0.125f; m1 *= 0.125f;
#pragma unroll
      for (int w = 0; w < NW; ++w) { v0 = fmaf(64.f * (st[w][0] - m0), st[w][0] - m0, v0); v1 = fmaf(64.f * (st[w][2] - m1), st[w][2] - m1, v1); }
      const float d0 = rbf(sqrtf(rbf(rbf(v0 * (1.f / 512.f)) + 1e-5f))), d1 = rbf(sqrtf(rbf(rbf(v1 * (1.f / 512.f)) + 1e-5f)));
      p_ln[tid] = (u16)f2bf(rbf(h0 - rbf(m0)) / d0);
      p_ln[512 + tid] = (u16)f2bf(rbf(h1 - rbf(m1)) / d1);
    }
    FENCE();
    {
      const u16* ap = p_ln + (c < 2 ? c : 2) * 512 + 64 * wave + 8 * r;
      const bf16x8 a0 = *reinterpret_cast<const bf16x8*>(ap), a1 = *reinterpret_cast<const bf16x8*>(ap + 32);
      f32x4 Dg = {0.f, 0.f, 0.f, 0.f}, Du = {0.f, 0.f, 0.f, 0.f};
      Dg = __builtin_amdgcn_mfma_f32_16x16x32_bf16(a0, GB[0][0], Dg, 0, 0, 0);
      Du = __builtin_amdgcn_mfma_f32_16x16x32_bf16(a0, GB[1][0], Du, 0, 0, 0);
      Dg = __builtin_amdgcn_mfma_f32_16x16x32_bf16(a1, GB[0][1], Dg, 0, 0, 0);
      Du = __builtin_amdgcn_mfma_f32_16x16x32_bf16(a1, GB[1][1], Du, 0, 0, 0);
      if (lane < 16) *reinterpret_cast<f32x4*>(lds + L_QP + 64 + (wave * 16 + lane) * 4) = f32x4{Dg[0], Dg[1], Du[0], Du[1]};
    }
    __syncthreads();
    if (tid < 64) {
      float g = 0.f, u = 0.f;
#pragma unroll
      for (int w = 0; w < NW; ++w) { g += lds[L_QP + 64 + (w * 16 + c) * 4 + (r & 1)]; u += lds[L_QP + 64 + (w * 16 + c) * 4 + 2 + (r & 1)]; }
      g = rbf(g); u = rbf(u);
      const float sg = rbf(g / (1.f + __expf(-g)));
      const u32 av = f2bf(sg * u);
      auto t = __builtin_amdgcn_permlane16_swap(av, av, false, false);
      const u32 pk = t[0] | (t[1] << 16);
      st_gran(ws + WS_AG + r * FF + rcol0 + c, tag, pk);
      st_gran(ws + WS_AG + (r + 4) * FF + rcol0 + c, tag, pk);
    }
  } else if (role == 0) {
    const u64* ap = ws + WS_AG + xcd * FF + tid;
    const u64* pp[5] = {ap, ap + 512, ap + 1024, tid < 256 ? ap + 1536 : ap, ws + WS_HG + xcd * 512 + rcol0 + (tid & 7)};
    u32 v[5];
    poll_gran<5>(pp, tag, v);
    f32x2 a0[4], a1[4];
#pragma unroll
    for (int k = 0; k < 4; ++k) { a0[k] = f32x2{0.f, 0.f}; a1[k] = f32x2{0.f, 0.f}; }
#pragma unroll
    for (int i = 0; i < 4; ++i) {
      float l0 = lo2f(v[i]), l1 = hi2f(v[i]);
      if (i == 3 && tid >= 256) { l0 = 0.f; l1 = 0.f; }
      fma8(a0, l0, RW[i]);
      fma8(a1, l1, RW[i]);
    }
    float y[8];
#pragma unroll
    for (int k = 0; k < 4; ++k) { y[2 * k] = red32(a0[k][0], a1[k][0]); y[2 * k + 1] = red32(a0[k][1], a1[k][1]); }
#pragma unroll
    for (int k = 0; k < 4; ++k) y[k] = red16(y[k], y[k + 4]);
#pragma unroll
    for (int k = 0; k < 2; ++k) y[k] = redd<ROR8>(y[k], y[k + 2], c & 8);
    y[0] = redd<XOR7>(y[0], y[1], c & 4);
    y[0] += dpp<XOR2>(y[0]);
    y[0] += dpp<XOR1>(y[0]);
    if ((lane & 3) == 0) red[wave * 16 + (lane >> 2)] = y[0];
    __syncthreads();
    if (tid < 16) {
      float s = 0.f;
#pragma unroll
      for (int w = 0; w < NW; ++w) s += red[w * 16 + tid];
      float hh = (tid < 8) ? lo2f(v[4]) : hi2f(v[4]);
      out[(tid >> 3) * DM + rcol0 + (tid & 7)] = (u16)f2bf(hh + rbf(s));
    }
    if (bid == 0 && tid == 0) __hip_atomic_store((u32*)(ws + WS_E), tag, __ATOMIC_RELAXED, __HIP_MEMORY_SCOPE_AGENT);
  }
}

extern "C" void kernel_launch(void* const* d_in, const int* in_sizes, int n_in,
                              void* d_out, int out_size, void* d_ws, size_t ws_size,
                              hipStream_t stream) {
  fused_kernel<<<256, NTHR, 0, stream>>>((const u16*)d_in[0], (const u16*)d_in[1], (const u16*)d_in[2], (const u16*)d_in[3],
                                         (const u16*)d_in[4], (const u16*)d_in[6], (const u16*)d_in[7], (const u16*)d_in[8],
                                         (u16*)d_out, (u64*)d_ws);
}
```
